```python
import torch

def _softmax(x, axis=-1):
    return torch.softmax(x, dim=axis)
def _gelu(x):
    return torch.nn.functional.gelu(x, approximate="tanh")
def _silu(x):
    return torch.nn.functional.silu(x)
def _sigmoid(x):
    return torch.sigmoid(x)
def _layernorm(x, axis=-1, eps=1e-5):
    m = x.mean(dim=axis, keepdim=True)
    v = x.var(dim=axis, keepdim=True, unbiased=False)
    return (x - m) / torch.sqrt(v + eps)
def _scatter_add(tgt, idx, src, axis=0):
    return tgt.clone().index_add_(axis, idx, src)
def _segment_sum(src, seg, n):
    out = torch.zeros((n,) + src.shape[1:], dtype=src.dtype)
    return out.index_add_(0, seg, src)
def _pad(x, pw):
    flat = tuple(v for pair in reversed(pw) for v in pair)
    return torch.nn.functional.pad(x, flat)

kernel_name = 'recurrent_scan__sliding_block__glu_gated_swiglu__prompt'

def setup_inputs():

    _w = (lambda a: (lambda t: t.to(torch.bfloat16) if t.is_floating_point() else t)(torch.as_tensor(a)))
    def _A_setup():
        B, S, D = 1, 2048, 512
        return {
            "x": _w((lambda *s: torch.randn(*s, dtype=torch.bfloat16) * 0.05)(B, S, D)),
            "wqkv": _w((lambda *s: torch.randn(*s, dtype=torch.bfloat16) * 0.2)(D, 3 * D)),
            "wo": _w((lambda *s: torch.randn(*s, dtype=torch.bfloat16) * 0.2)(D, D)),
        }
    _A_in = _A_setup()

    def _B_setup():
        B, S, D, F = 1, 2048, 512, 1376
        return {
            "x": _w((lambda *s: torch.randn(*s, dtype=torch.bfloat16) * 0.05)(B, S, D)),
            "w1": _w((lambda *s: torch.randn(*s, dtype=torch.bfloat16) * 0.1)(D, F) * (2.0 / D) ** 0.5),
            "w3": _w((lambda *s: torch.randn(*s, dtype=torch.bfloat16) * 0.1)(D, F) * (2.0 / D) ** 0.5),
            "w2": _w((lambda *s: torch.randn(*s, dtype=torch.bfloat16) * 0.1)(F, D) * (2.0 / F) ** 0.5),
        }
    _B_in = _B_setup()

    return {
        "A_x": _A_in["x"],
        "A_wqkv": _A_in["wqkv"],
        "A_wo": _A_in["wo"],
        "B_x": _B_in["x"],
        "B_w1": _B_in["w1"],
        "B_w3": _B_in["w3"],
        "B_w2": _B_in["w2"],
    }

def reference(A_x, A_wqkv, A_wo, B_x, B_w1, B_w3, B_w2):
    def _A_ref(x, wqkv, wo):
        B, S, D = x.shape
        H = 32; Dh = D // H; W = 256
        qkv = (lambda x, *d: x.permute(*d))((x @ wqkv).reshape(B, S, 3, H, Dh), 2, 0, 3, 1, 4)
        q, k, v = qkv[0], qkv[1], qkv[2]
        scores = torch.einsum("bhqd,bhkd->bhqk", q, k) * (Dh ** -0.5)
        idx = torch.arange(S)
        mask = (idx[:, None] - idx[None, :] >= 0) & (idx[:, None] - idx[None, :] < W)
        scores = torch.where(mask[None, None], scores, -1e9)
        p = _softmax(scores, axis=-1)
        o = torch.einsum("bhqk,bhkd->bhqd", p, v)
        return (lambda x, *d: x.permute(*d))(o, 0, 2, 1, 3).reshape(B, S, D) @ wo

    def _B_ref(x, w1, w3, w2):
        gate = torch.einsum("bsd,df->bsf", x, w1)
        up = torch.einsum("bsd,df->bsf", x, w3)
        h = _silu(gate) * up
        out = torch.einsum("bsf,fd->bsd", h, w2)
        return out

    x = A_x
    xn = _layernorm(x, axis=-1)
    s = _A_ref(xn, A_wqkv, A_wo)
    g = _silu(_B_ref(xn, B_w1, B_w3, B_w2))
    return x + s * g
```

```cpp
#include <hip/hip_runtime.h>
#include <hip/hip_bf16.h>
#include <stdint.h>

typedef __hip_bfloat16 bf16;
static constexpr int S = 2048, D = 512, H = 32, DH = 16, W = 256, F = 1376;

__device__ __forceinline__ float b2f(bf16 v) { return __bfloat162float(v); }
__device__ __forceinline__ bf16 f2b(float v) { return __float2bfloat16(v); }
__device__ __forceinline__ float rbf(float v) { return b2f(f2b(v)); }

__global__ void k_ln(const bf16* x, bf16* xn) {
    int row = blockIdx.x;
    int lane = threadIdx.x;
    float s = 0.f;
    for (int i = lane; i < D; i += 64) s += b2f(x[row * D + i]);
    for (int o = 32; o; o >>= 1) s += __shfl_xor(s, o);
    float mean = s / D;
    float v = 0.f;
    for (int i = lane; i < D; i += 64) { float d = b2f(x[row * D + i]) - mean; v += d * d; }
    for (int o = 32; o; o >>= 1) v += __shfl_xor(v, o);
    v /= D;
    float mb = rbf(mean);
    float vb = rbf(v);
    float ve = rbf(vb + 1e-5f);
    float sd = rbf(sqrtf(ve));
    for (int i = lane; i < D; i += 64) {
        float d = rbf(b2f(x[row * D + i]) - mb);
        xn[row * D + i] = f2b(d / sd);
    }
}

__global__ void k_gemm(const bf16* A, const bf16* B, bf16* C, int M, int N, int K) {
    int n = blockIdx.x * blockDim.x + threadIdx.x;
    int m = blockIdx.y;
    if (n >= N) return;
    float acc = 0.f;
    for (int k = 0; k < K; ++k) acc += b2f(A[m * K + k]) * b2f(B[k * N + n]);
    C[m * N + n] = f2b(acc);
}

__global__ void k_swiglu(const bf16* g, const bf16* u, bf16* h, int n) {
    int i = blockIdx.x * blockDim.x + threadIdx.x;
    if (i >= n) return;
    float gv = b2f(g[i]);
    float sg = rbf(gv / (1.f + __expf(-gv)));
    h[i] = f2b(sg * b2f(u[i]));
}

__global__ void k_attn(const bf16* qkv, bf16* o) {
    int q = blockIdx.x, h = blockIdx.y;
    int lane = threadIdx.x;
    __shared__ float sc[W];
    int k0 = q - W + 1; if (k0 < 0) k0 = 0;
    int nk = q - k0 + 1;
    float mx = -1e30f;
    for (int j = lane; j < nk; j += 64) {
        int key = k0 + j;
        float acc = 0.f;
        for (int d = 0; d < DH; ++d) acc += b2f(qkv[q * 3 * D + h * DH + d]) * b2f(qkv[key * 3 * D + D + h * DH + d]);
        float s = rbf(rbf(acc) * 0.25f);
        sc[j] = s;
        mx = fmaxf(mx, s);
    }
    for (int of = 32; of; of >>= 1) mx = fmaxf(mx, __shfl_xor(mx, of));
    float sum = 0.f;
    for (int j = lane; j < nk; j += 64) { float e = __expf(sc[j] - mx); sc[j] = e; sum += e; }
    for (int of = 32; of; of >>= 1) sum += __shfl_xor(sum, of);
    __syncthreads();
    if (lane < DH) {
        float acc = 0.f;
        for (int j = 0; j < nk; ++j) {
            float p = rbf(sc[j] / sum);
            acc += p * b2f(qkv[(k0 + j) * 3 * D + 2 * D + h * DH + lane]);
        }
        o[q * D + h * DH + lane] = f2b(acc);
    }
}

__global__ void k_final(const bf16* x, const bf16* s, const bf16* f, bf16* out, int n) {
    int i = blockIdx.x * blockDim.x + threadIdx.x;
    if (i >= n) return;
    float fv = b2f(f[i]);
    float g = rbf(fv / (1.f + __expf(-fv)));
    float sg = rbf(b2f(s[i]) * g);
    out[i] = f2b(b2f(x[i]) + sg);
}

extern "C" void kernel_launch(void* const* d_in, const int* in_sizes, int n_in,
                              void* d_out, int out_size, void* d_ws, size_t ws_size,
                              hipStream_t stream) {
    const bf16* x = (const bf16*)d_in[0];
    const bf16* wqkv = (const bf16*)d_in[1];
    const bf16* wo = (const bf16*)d_in[2];
    const bf16* w1 = (const bf16*)d_in[4];
    const bf16* w3 = (const bf16*)d_in[5];
    const bf16* w2 = (const bf16*)d_in[6];
    bf16* out = (bf16*)d_out;
    char* ws = (char*)d_ws;
    bf16* xn = (bf16*)ws; ws += (size_t)S * D * 2;
    bf16* qkv = (bf16*)ws; ws += (size_t)S * 3 * D * 2;
    bf16* g = (bf16*)ws; ws += (size_t)S * F * 2;
    bf16* u = (bf16*)ws; ws += (size_t)S * F * 2;
    bf16* h = (bf16*)ws; ws += (size_t)S * F * 2;
    bf16* o = (bf16*)ws; ws += (size_t)S * D * 2;
    bf16* s = (bf16*)ws; ws += (size_t)S * D * 2;
    bf16* f = (bf16*)ws; ws += (size_t)S * D * 2;

    k_ln<<<S, 64, 0, stream>>>(x, xn);
    k_gemm<<<dim3(3 * D / 256, S), 256, 0, stream>>>(xn, wqkv, qkv, S, 3 * D, D);
    k_gemm<<<dim3((F + 255) / 256, S), 256, 0, stream>>>(xn, w1, g, S, F, D);
    k_gemm<<<dim3((F + 255) / 256, S), 256, 0, stream>>>(xn, w3, u, S, F, D);
    k_swiglu<<<(S * F + 255) / 256, 256, 0, stream>>>(g, u, h, S * F);
    k_attn<<<dim3(S, H), 64, 0, stream>>>(qkv, o);
    k_gemm<<<dim3(D / 256, S), 256, 0, stream>>>(o, wo, s, S, D, D);
    k_gemm<<<dim3(D / 256, S), 256, 0, stream>>>(h, w2, f, S, D, F);
    k_final<<<(S * D + 255) / 256, 256, 0, stream>>>(x, s, f, out, S * D);
}
```

```cpp
#include <hip/hip_runtime.h>
#include <hip/hip_bf16.h>
#include <stdint.h>

typedef __attribute__((ext_vector_type(8))) short s16x8;
typedef __attribute__((ext_vector_type(4))) short s16x4;
typedef __attribute__((ext_vector_type(16))) float f32x16;
typedef __attribute__((ext_vector_type(4))) float f32x4;
typedef __attribute__((ext_vector_type(4))) unsigned u32x4;
typedef __attribute__((ext_vector_type(2))) __bf16 bf16x2_t;
typedef __attribute__((ext_vector_type(2))) float f32x2;
#define LAS __attribute__((address_space(3)))
#define GAS __attribute__((address_space(1)))
#define DEVINL __device__ __forceinline__

template <int I> struct IC { static constexpr int value = I; constexpr operator int() const { return I; } };
template <int B, int E, class F> DEVINL void static_for(F&& f) { if constexpr (B < E) { f(IC<B>{}); static_for<B + 1, E>(f); } }

template <int N> DEVINL void wait_vm() { asm volatile("s_waitcnt vmcnt(%0)" ::"n"(N) : "memory"); }
DEVINL void wg_barrier() { asm volatile("s_waitcnt lgkmcnt(0)\n\ts_barrier" ::: "memory"); }

DEVINL void glds16(const void* gsrc, LAS void* ldst) {
    __builtin_amdgcn_global_load_lds((const GAS void*)gsrc, ldst, 16, 0, 0);
}
typedef __attribute__((ext_vector_type(4))) int rsrc_t;
DEVINL rsrc_t mk_rsrc(const void* p, unsigned nbytes) {
    unsigned long long a = (unsigned long long)p;
    rsrc_t r = {(int)(unsigned)a, (int)(unsigned)(a >> 32), (int)nbytes, 0x00020000};
    return r;
}
DEVINL void blds16(rsrc_t r, LAS void* ldst, int voff, int soff) {
    unsigned keep;
    asm volatile("s_mov_b32 %0, m0\n\ts_mov_b32 m0, %1\n\ts_nop 4\n\tbuffer_load_dwordx4 %2, %3, %4 offen lds\n\ts_mov_b32 m0, %0"
                 : "=&s"(keep) : "s"(__builtin_amdgcn_readfirstlane((unsigned)(size_t)ldst)), "v"(voff), "s"(r), "s"(__builtin_amdgcn_readfirstlane(soff)) : "memory");
}
DEVINL f32x16 mfma32(s16x8 a, s16x8 b, f32x16 c) {
    return __builtin_amdgcn_mfma_f32_32x32x16_bf16(__builtin_bit_cast(__attribute__((ext_vector_type(8))) __bf16, a),
                                                    __builtin_bit_cast(__attribute__((ext_vector_type(8))) __bf16, b), c, 0, 0, 0);
}
DEVINL s16x4 tr_read(const LAS char* p) { return __builtin_amdgcn_ds_read_tr16_b64_v4i16((LAS s16x4*)p); }
DEVINL s16x8 wfrag(const LAS char* p0, const LAS char* p1) {
    s16x4 a = tr_read(p0), b = tr_read(p1);
    return __builtin_shufflevector(a, b, 0, 1, 2, 3, 4, 5, 6, 7);
}
DEVINL unsigned pack_bf16(float lo, float hi) {
    f32x2 v = {lo, hi};
    bf16x2_t b = __builtin_convertvector(v, bf16x2_t);
    return __builtin_bit_cast(unsigned, b);
}
DEVINL float dot2_acc(unsigned a, unsigned b, float c) {
    return __builtin_amdgcn_fdot2_f32_bf16(__builtin_bit_cast(bf16x2_t, a), __builtin_bit_cast(bf16x2_t, b), c, false);
}
#define ONES2 0x3F803F80u

static constexpr int S = 2048, D = 512, H = 32, F = 1376;
static constexpr int NTT = 64;
static constexpr int NCH = 118;

static constexpr int SLOT = 16384;
static constexpr int RING = 8 * SLOT;
static constexpr int SCR_CSP = RING;
static constexpr int SCR_WV = RING + 2048;
static constexpr int K1_LDS = RING + 2048 + 4096;

template <int CH> constexpr int item_cnt(int i, int np) {
    if (i < 16) return (i & 1) ? (CH == 0 ? 2 : 1) : 2;
    if (i < 24) return 2;
    if (np == 3 && i < 32) return 1;
    return 0;
}
template <int CH> constexpr int younger(int first, int last, int np) {
    int n = 0;
    for (int i = first; i <= last; ++i) n += item_cnt<CH>(i, np);
    return n;
}

struct K1Ctx {
    rsrc_t rx, rq, rf;
    char* Qf; char* Kf; char* Vf; char* actf;
    int rb, hp, pair0, np;
    int w, rg, lane;
    int voff_x, voff_qkv, voff_ffn;
};

DEVINL void k1_issue_x(const K1Ctx& c, LAS char* sm, int s, int slot) {
#pragma unroll
    for (int m = 0; m < 2; ++m) {
        int id = c.w + 8 * m;
        blds16(c.rx, sm + slot * SLOT + id * 1024, c.voff_x, (c.rb * 128 + 8 * id) * 1024 + s * 128);
    }
}
DEVINL void k1_issue_wqkv(const K1Ctx& c, LAS char* sm, int s, int slot) {
#pragma unroll
    for (int m = 0; m < 2; ++m) {
        int id = c.w + 8 * m;
        if (id < 12) {
            int tile = id >> 2, rb16 = id & 3;
            blds16(c.rq, sm + slot * SLOT + id * 1024, c.voff_qkv, (64 * s + 16 * rb16) * 3072 + (tile * 512 + c.hp * 32) * 2);
        }
    }
}
DEVINL void k1_issue_ffn(const K1Ctx& c, LAS char* sm, int round, int j, int slot) {
    int rb16 = c.w & 3;
    int ntiles = (2 * round + 1 < c.np) ? 4 : 2;
#pragma unroll
    for (int m = 0; m < 2; ++m) {
        if (m * 2 < ntiles) {
            int pair = c.pair0 + 2 * round + m;
            blds16(c.rf, sm + slot * SLOT + (c.w + 8 * m) * 1024, c.voff_ffn, (64 * j + 16 * rb16) * 2752 + pair * 64);
        }
    }
}

DEVINL void k1_load_cs(LAS char* sm, int wvoff, int csp_tile, int lane, f32x4 (&c4)[4]) {
    LAS float* csp = (LAS float*)(sm + SCR_CSP) + csp_tile * 128;
    LAS float* csf = (LAS float*)(sm + wvoff);
    int n = lane & 31;
    float v = (csp[n] + csp[32 + n]) + (csp[64 + n] + csp[96 + n]);
    if (lane < 32) csf[n] = v;
    asm volatile("" ::: "memory");
    int h = lane >> 5;
#pragma unroll
    for (int g = 0; g < 4; ++g) c4[g] = *(LAS f32x4*)(csf + 8 * g + 4 * h);
}

DEVINL void store_frag16(char* p, const float* v) {
    u32x4 o = {pack_bf16(v[0], v[1]), pack_bf16(v[2], v[3]), pack_bf16(v[4], v[5]), pack_bf16(v[6], v[7])};
    *(u32x4*)p = o;
}

template <int CH>
DEVINL void k1_role(const K1Ctx& c, LAS char* sm) {
    const int lane = c.lane, rg = c.rg, h = lane >> 5;
    const int wl = (8 * h + ((lane & 15) >> 2)) * 64 + ((lane >> 4) & 1) * 32 + (lane & 3) * 8;
    const int xr = 32 * rg + (lane & 31);
    const int xm = (xr >> 1) & 7;
    const int xa0 = xr * 128 + ((h ^ (xm & 1)) << 4) + ((xm >> 1) << 5);
    const int wvoff = SCR_WV + (CH * 4 + rg) * 512;

    s16x8 xf[32];
    float s1 = 0.f, s2 = 0.f;
    f32x16 accA = {0}, accB = {0};
    float csA = 0.f, csB = 0.f;

    static_for<0, 8>([&](auto SI) {
        constexpr int s = SI;
        constexpr int N = younger<CH>(2 * s + 2, s == 0 ? 7 : 2 * s + 5, 3);
        wait_vm<N>();
        wg_barrier();
        if constexpr (s >= 1) {
            static_for<0, 2>([&](auto QI) {
                constexpr int it = 2 * s + 6 + QI;
                if constexpr (it < 16) {
                    if constexpr ((it & 1) == 0) k1_issue_x(c, sm, it >> 1, it & 7);
                    else k1_issue_wqkv(c, sm, it >> 1, it & 7);
                } else {
                    k1_issue_ffn(c, sm, 0, it - 16, it & 7);
                }
            });
        }
        const LAS char* xs = sm + ((2 * s) & 7) * SLOT;
        const LAS char* wsl = sm + ((2 * s + 1) & 7) * SLOT;
        static_for<0, 4>([&](auto KI) {
            constexpr int ks = KI;
            s16x8 xv = *(const LAS s16x8*)(xs + (xa0 ^ (ks << 5)));
            xf[4 * s + ks] = xv;
            u32x4 xu = __builtin_bit_cast(u32x4, xv);
#pragma unroll
            for (int q = 0; q < 4; ++q) { s1 = dot2_acc(xu[q], ONES2, s1); s2 = dot2_acc(xu[q], xu[q], s2); }
            if constexpr (CH == 0) {
                s16x8 wv = wfrag(wsl + 2 * 4096 + wl + ks * 1024, wsl + 2 * 4096 + wl + ks * 1024 + 256);
                accA = mfma32(xv, wv, accA);
            } else {
                s16x8 wq = wfrag(wsl + wl + ks * 1024, wsl + wl + ks * 1024 + 256);
                s16x8 wk = wfrag(wsl + 4096 + wl + ks * 1024, wsl + 4096 + wl + ks * 1024 + 256);
                accA = mfma32(wq, xv, accA);
                accB = mfma32(wk, xv, accB);
            }
        });
        if constexpr (CH == 0) {
            u32x4 wv = __builtin_bit_cast(u32x4, wfrag(wsl + 2 * 4096 + wl + rg * 1024, wsl + 2 * 4096 + wl + rg * 1024 + 256));
#pragma unroll
            for (int q = 0; q < 4; ++q) csA = dot2_acc(wv[q], ONES2, csA);
        } else {
            u32x4 wq = __builtin_bit_cast(u32x4, wfrag(wsl + wl + rg * 1024, wsl + wl + rg * 1024 + 256));
            u32x4 wk = __builtin_bit_cast(u32x4, wfrag(wsl + 4096 + wl + rg * 1024, wsl + 4096 + wl + rg * 1024 + 256));
#pragma unroll
            for (int q = 0; q < 4; ++q) { csA = dot2_acc(wq[q], ONES2, csA); csB = dot2_acc(wk[q], ONES2, csB); }
        }
    });

    s1 += __shfl_xor(s1, 32);
    s2 += __shfl_xor(s2, 32);
    const float mean = s1 * (1.f / 512.f);
    float var = s2 * (1.f / 512.f) - mean * mean;
    var = fmaxf(var, 0.f);
    const float rstd = 1.0f / sqrtf(var + 1e-5f);

    LAS float* csp = (LAS float*)(sm + SCR_CSP);
    {
        float pa = csA + __shfl_xor(csA, 32);
        float pb = csB + __shfl_xor(csB, 32);
        if (lane < 32) {
            if constexpr (CH == 0) { csp[2 * 128 + rg * 32 + lane] = pa; }
            else { csp[0 * 128 + rg * 32 + lane] = pa; csp[1 * 128 + rg * 32 + lane] = pb; }
        }
        if constexpr (CH == 0) {
            LAS float* AB = (LAS float*)(sm + wvoff + 256);
            if (lane < 32) { AB[lane] = rstd; AB[32 + lane] = -mean * rstd; }
        }
    }

    const int tt = 4 * c.rb + rg;
    const int nrounds = (c.np + 1) >> 1;

    {
        constexpr int NF = younger<CH>(17, 21, 3);
        wait_vm<NF>();
        wg_barrier();
        k1_issue_ffn(c, sm, 0, 6, 6);
        k1_issue_ffn(c, sm, 0, 7, 7);
                    if constexpr (CH == 0) {
                        LAS float* cspv = (LAS float*)(sm + SCR_CSP) + 2 * 128;
                        int n = lane & 31;
                        float csV = (cspv[n] + cspv[32 + n]) + (cspv[64 + n] + cspv[96 + n]);
                        LAS float* AB = (LAS float*)(sm + wvoff + 256);
                        float v[16];
#pragma unroll
                        for (int g = 0; g < 4; ++g) {
                            f32x4 A4 = *(LAS f32x4*)(AB + 8 * g + 4 * h);
                            f32x4 B4 = *(LAS f32x4*)(AB + 32 + 8 * g + 4 * h);
#pragma unroll
                            for (int i = 0; i < 4; ++i) v[4 * g + i] = fmaf(accA[4 * g + i], A4[i], csV * B4[i]);
                        }
                        int head = 2 * c.hp + (n >> 4);
                        char* vp = c.Vf + (size_t)((head * NTT + tt) * 2) * 512 + (h * 16 + (n & 15)) * 16;
                        store_frag16(vp, v);
                        store_frag16(vp + 512, v + 8);
                    } else {
                        static_for<0, 2>([&](auto TI) {
                            constexpr int t = TI;
                            f32x4 c4[4];
                            k1_load_cs(sm, wvoff + 128 * t, t, lane, c4);
                            const f32x16& acc = t == 0 ? accA : accB;
                            float v[16];
#pragma unroll
                            for (int g = 0; g < 4; ++g)
#pragma unroll
                                for (int i = 0; i < 4; ++i) v[4 * g + i] = rstd * fmaf(-mean, c4[g][i], acc[4 * g + i]);
                            char* base = (t == 0 ? c.Qf : c.Kf);
                            store_frag16(base + (size_t)((2 * c.hp) * NTT + tt) * 1024 + lane * 16, v);
                            store_frag16(base + (size_t)((2 * c.hp + 1) * NTT + tt) * 1024 + lane * 16, v + 8);
                        });
                    }
    }
    for (int r = 0; r < nrounds; ++r) {
        const bool active = (2 * r + CH) < c.np;
        const int tbl = (r == 0) ? (c.np == 3 ? 0 : 1) : 2;
        f32x16 accG = {0}, accU = {0};
        float csG = 0.f, csU = 0.f;
        static_for<0, 8>([&](auto JI) {
            constexpr int j = JI;
            constexpr int N0 = younger<CH>(16 + j + 1, j == 0 ? 21 : 16 + j + 6, 3);
            constexpr int N1 = younger<CH>(16 + j + 1, j == 0 ? 21 : 16 + j + 6, 2);
            constexpr int N2 = younger<CH>(24 + j + 1, 24 + j + 6, 3);
            if constexpr (j == 0) {
                if (r > 0) {
                    wait_vm<N2>();
                    wg_barrier();
                    k1_issue_ffn(c, sm, r, 7, 7);
                }
            } else {
                if (tbl == 0) wait_vm<N0>(); else if (tbl == 1) wait_vm<N1>(); else wait_vm<N2>();
                wg_barrier();
                if (r + 1 < nrounds) k1_issue_ffn(c, sm, r + 1, j - 1, j - 1);
            }
            if (active) {
                const LAS char* wsl = sm + j * SLOT + (CH * 2) * 4096;
                static_for<0, 4>([&](auto KI) {
                    constexpr int ks = KI;
                    s16x8 wg = wfrag(wsl + wl + ks * 1024, wsl + wl + ks * 1024 + 256);
                    s16x8 wu = wfrag(wsl + 4096 + wl + ks * 1024, wsl + 4096 + wl + ks * 1024 + 256);
                    accG = mfma32(wg, xf[4 * j + ks], accG);
                    accU = mfma32(wu, xf[4 * j + ks], accU);
                });
                u32x4 wg = __builtin_bit_cast(u32x4, wfrag(wsl + wl + rg * 1024, wsl + wl + rg * 1024 + 256));
                u32x4 wu = __builtin_bit_cast(u32x4, wfrag(wsl + 4096 + wl + rg * 1024, wsl + 4096 + wl + rg * 1024 + 256));
#pragma unroll
                for (int q = 0; q < 4; ++q) { csG = dot2_acc(wg[q], ONES2, csG); csU = dot2_acc(wu[q], ONES2, csU); }
            }
        });
        if (active) {
            float pa = csG + __shfl_xor(csG, 32);
            float pb = csU + __shfl_xor(csU, 32);
            if (lane < 32) { csp[(CH * 2) * 128 + rg * 32 + lane] = pa; csp[(CH * 2 + 1) * 128 + rg * 32 + lane] = pb; }
        }
        wg_barrier();
        if (active) {
            f32x4 cg4[4], cu4[4];
            k1_load_cs(sm, wvoff, CH * 2, lane, cg4);
            k1_load_cs(sm, wvoff + 128, CH * 2 + 1, lane, cu4);
            float v[16];
#pragma unroll
            for (int g = 0; g < 4; ++g)
#pragma unroll
                for (int i = 0; i < 4; ++i) {
                    float gv = rstd * fmaf(-mean, cg4[g][i], accG[4 * g + i]);
                    float uv = rstd * fmaf(-mean, cu4[g][i], accU[4 * g + i]);
                    float e = __builtin_amdgcn_exp2f(gv * -1.4426950408889634f);
                    float sg = gv * __builtin_amdgcn_rcpf(1.f + e);
                    v[4 * g + i] = sg * uv;
                }
            int pair = c.pair0 + 2 * r + CH;
            char* hp0 = c.actf + (size_t)(tt * NCH + 2 * pair) * 1024 + lane * 16;
            store_frag16(hp0, v);
            store_frag16(hp0 + 1024, v + 8);
        }
    }
}

__global__ __launch_bounds__(512, 2) void k1_qkv_ffn(const char* __restrict__ x, const char* __restrict__ wqkv, const char* __restrict__ w1,
                                                     const char* __restrict__ w3, char* Qf, char* Kf, char* Vf, char* actf) {
    extern __shared__ __attribute__((aligned(16))) char smem[];
    LAS char* sm = (LAS char*)smem;
    K1Ctx c;
    c.Qf = Qf; c.Kf = Kf; c.Vf = Vf; c.actf = actf;
    int b = blockIdx.x, xcd = b & 7, j = b >> 3;
    c.rb = (xcd >> 2) * 8 + (j & 7);
    int cg = (xcd & 3) * 4 + (j >> 3);
    c.hp = cg;
    c.np = cg < 11 ? 3 : 2;
    c.pair0 = cg < 11 ? 3 * cg : 33 + 2 * (cg - 11);
    c.w = __builtin_amdgcn_readfirstlane(threadIdx.x >> 6);
    c.rg = c.w & 3;
    c.lane = threadIdx.x & 63;
    c.voff_qkv = (c.lane >> 2) * 3072 + (c.lane & 3) * 16;
    c.voff_ffn = (c.lane >> 2) * 2752 + (c.lane & 3) * 16;
    c.voff_x = (c.lane >> 3) * 1024 + (((c.lane & 7) ^ ((4 * (c.w & 1) + (c.lane >> 4)) & 7)) << 4);
    c.rx = mk_rsrc(x, S * D * 2);
    c.rq = mk_rsrc(wqkv, D * 3 * D * 2);
    c.rf = mk_rsrc((c.w >> 2) ? w3 : w1, D * F * 2);
#pragma unroll
    for (int s = 0; s < 4; ++s) { k1_issue_x(c, sm, s, 2 * s); k1_issue_wqkv(c, sm, s, 2 * s + 1); }
    if (c.w < 4) k1_role<0>(c, sm); else k1_role<1>(c, sm);
}

static constexpr int K2_K = 0, K2_V = 24576, K2_ONES = 49152, K2_LDS = 49152 + 12288;

__global__ __launch_bounds__(512, 2) void k2_attn(const char* __restrict__ Qf, const char* __restrict__ Kf, const char* __restrict__ Vf, char* actf) {
    extern __shared__ __attribute__((aligned(16))) char smem[];
    LAS char* sm = (LAS char*)smem;
    const int b = blockIdx.x, xcd = b & 7, jj = b >> 3;
    const int hp = 2 * xcd + (jj >> 4), qb = jj & 15;
    const int w = __builtin_amdgcn_readfirstlane(threadIdx.x >> 6), lane = threadIdx.x & 63;
    const int hl = w >> 2, qi = w & 3, head = 2 * hp + hl, qt = 4 * qb + qi;
    const int h = lane >> 5;
    {
        rsrc_t rk = mk_rsrc(Kf, 2u << 20), rv = mk_rsrc(Vf, 2u << 20);
#pragma unroll
        for (int m = 0; m < 6; ++m) {
            int id = w + 8 * m;
            int arr = id / 24, hh = (id % 24) / 12, t = id % 12;
            int tt = 4 * qb - 8 + t; if (tt < 0) tt = 0;
            blds16(arr ? rv : rk, sm + arr * 24576 + (hh * 12 + t) * 1024, lane * 16, ((2 * hp + hh) * NTT + tt) * 1024);
        }
    }
    s16x8 qf = *(const s16x8*)(Qf + (size_t)(head * NTT + qt) * 1024 + lane * 16);
    {
        u32x4 ones = {ONES2, ONES2, ONES2, ONES2};
        *(LAS u32x4*)(sm + K2_ONES + threadIdx.x * 16) = ones;
        if (threadIdx.x < 256) *(LAS u32x4*)(sm + K2_ONES + 8192 + threadIdx.x * 16) = ones;
    }
    wait_vm<0>();
    wg_barrier();

    f32x16 s[9];
    const LAS char* kb = sm + K2_K + (hl * 12 + qi) * 1024 + lane * 16;
    static_for<0, 9>([&](auto TI) {
        constexpr int t = TI;
        s16x8 kf = *(const LAS s16x8*)(kb + t * 1024);
        f32x16 z = {0};
        s[t] = mfma32(kf, qf, z);
    });
    const int ql = lane & 31;
    const float NEG = -__builtin_inff();
#pragma unroll
    for (int r = 0; r < 16; ++r) {
        int kl = (r & 3) + 8 * (r >> 2) + 4 * h;
        s[0][r] = (kl > ql) ? s[0][r] : NEG;
        s[8][r] = (kl <= ql) ? s[8][r] : NEG;
    }
    if (qt < 8) {
        static_for<0, 8>([&](auto TI) {
            constexpr int t = TI;
            if (qt - 8 + t < 0) {
#pragma unroll
                for (int r = 0; r < 16; ++r) s[t][r] = NEG;
            }
        });
    }
    float m = s[8][0];
    static_for<0, 9>([&](auto TI) {
        constexpr int t = TI;
#pragma unroll
        for (int r = 0; r < 16; ++r) m = fmaxf(m, s[t][r]);
    });
    m = fmaxf(m, __shfl_xor(m, 32));
    const float cs = 0.25f * 1.4426950408889634f;
    const float mc = -m * cs;
    f32x16 o = {0};
    const LAS char* vb = sm + ((lane & 16) ? K2_ONES : (K2_V + hl * 12288)) + qi * 1024 + (h * 16 + (lane & 15)) * 16;
    static_for<0, 9>([&](auto TI) {
        constexpr int t = TI;
        float p[16];
#pragma unroll
        for (int r = 0; r < 16; ++r) p[r] = __builtin_amdgcn_exp2f(fmaf(s[t][r], cs, mc));
#pragma unroll
        for (int k2 = 0; k2 < 2; ++k2) {
            u32x4 pk = {pack_bf16(p[8 * k2 + 0], p[8 * k2 + 1]), pack_bf16(p[8 * k2 + 2], p[8 * k2 + 3]),
                        pack_bf16(p[8 * k2 + 4], p[8 * k2 + 5]), pack_bf16(p[8 * k2 + 6], p[8 * k2 + 7])};
            s16x8 vf = *(const LAS s16x8*)(vb + t * 1024 + k2 * 512);
            o = mfma32(vf, __builtin_bit_cast(s16x8, pk), o);
        }
    });
    float inv = 1.f / o[8];
    float v[8];
#pragma unroll
    for (int r = 0; r < 8; ++r) v[r] = o[r] * inv;
    store_frag16(actf + (size_t)(qt * NCH + 86 + head) * 1024 + lane * 16, v);
}

static constexpr int K3_LDS = 8 * SLOT;

DEVINL void k3_issue(LAS char* sm, rsrc_t ra, rsrc_t rw2, rsrc_t rwo, int r64, int c64, int w, int voff_w, int lane, int st) {
    int slot = st & 7;
    int chunk0 = st < 22 ? 4 * st : 86 + 4 * (st - 22);
    blds16(ra, sm + slot * SLOT + w * 1024, lane * 16, ((2 * r64 + (w >> 2)) * NCH + chunk0 + (w & 3)) * 1024);
    int k0 = (st < 22 ? 64 * st : 64 * (st - 22)) + 8 * w;
    if (st == 21 && w >= 4) k0 = 64 * 21;
    blds16(st < 22 ? rw2 : rwo, sm + slot * SLOT + 8192 + w * 1024, voff_w, k0 * 1024 + c64 * 128);
}

__global__ __launch_bounds__(512, 2) void k3_out(const char* __restrict__ actf, const char* __restrict__ w2, const char* __restrict__ wo,
                                                 const char* __restrict__ x, char* __restrict__ out) {
    extern __shared__ __attribute__((aligned(16))) char smem[];
    LAS char* sm = (LAS char*)smem;
    const int b = blockIdx.x, xcd = b & 7, jj = b >> 3;
    const int r64 = 4 * xcd + (jj >> 3), c64 = jj & 7;
    const int w = __builtin_amdgcn_readfirstlane(threadIdx.x >> 6), lane = threadIdx.x & 63;
    const int ttl = w & 1, ct = (w >> 1) & 1, kh = w >> 2, h = lane >> 5;
    const rsrc_t ra = mk_rsrc(actf, NTT * NCH * 1024), rw2 = mk_rsrc(w2, F * D * 2), rwo = mk_rsrc(wo, D * D * 2);
    const int voff_w = (lane >> 3) * 1024 + (((lane & 7) ^ ((((lane >> 3) >> 1) & 1) << 2)) << 4);
#pragma unroll
    for (int st = 0; st < 7; ++st) k3_issue(sm, ra, rw2, rwo, r64, c64, w, voff_w, lane, st);
    const int q = (lane & 15) >> 2, p = lane & 3, grp = (lane >> 4) & 1;
    const int wl = 8192 + (4 * h + q) * 128 + (((ct * 4 + grp * 2 + (p >> 1)) ^ (((q >> 1) & 1) << 2)) << 4) + (p & 1) * 8;
    const int al = ttl * 4096 + lane * 16;
    f32x16 accF = {0}, accS = {0};
    for (int st = 0; st < 21; ++st) {
        wait_vm<12>();
        wg_barrier();
        k3_issue(sm, ra, rw2, rwo, r64, c64, w, voff_w, lane, st + 7);
        const LAS char* sl = sm + (st & 7) * SLOT;
#pragma unroll
        for (int k2 = 0; k2 < 2; ++k2) {
            int ks = 2 * kh + k2;
            s16x8 a = *(const LAS s16x8*)(sl + al + ks * 1024);
            s16x8 wf = wfrag(sl + wl + ks * 2048, sl + wl + ks * 2048 + 1024);
            accF = mfma32(wf, a, accF);
        }
    }
    {
        wait_vm<12>();
        wg_barrier();
        k3_issue(sm, ra, rw2, rwo, r64, c64, w, voff_w, lane, 28);
        const LAS char* sl = sm + (21 & 7) * SLOT;
        int ks = kh;
        s16x8 a = *(const LAS s16x8*)(sl + al + ks * 1024);
        s16x8 wf = wfrag(sl + wl + ks * 2048, sl + wl + ks * 2048 + 1024);
        accF = mfma32(wf, a, accF);
    }
    static_for<22, 30>([&](auto SI) {
        constexpr int st = SI;
        constexpr int N = st <= 23 ? 12 : 2 * (29 - st);
        wait_vm<N>();
        wg_barrier();
        if constexpr (st + 7 < 30) k3_issue(sm, ra, rw2, rwo, r64, c64, w, voff_w, lane, st + 7);
        const LAS char* sl = sm + (st & 7) * SLOT;
#pragma unroll
        for (int k2 = 0; k2 < 2; ++k2) {
            int ks = 2 * kh + k2;
            s16x8 a = *(const LAS s16x8*)(sl + al + ks * 1024);
            s16x8 wf = wfrag(sl + wl + ks * 2048, sl + wl + ks * 2048 + 1024);
            accS = mfma32(wf, a, accS);
        }
    });
    wg_barrier();
    {
        LAS f32x4* ex = (LAS f32x4*)(sm + ((w & 3) * 2 + kh) * 4096);
        const int ob = 8 * (1 - kh);
        f32x4 a0 = {accF[ob + 0], accF[ob + 1], accF[ob + 2], accF[ob + 3]};
        f32x4 a1 = {accF[ob + 4], accF[ob + 5], accF[ob + 6], accF[ob + 7]};
        f32x4 a2 = {accS[ob + 0], accS[ob + 1], accS[ob + 2], accS[ob + 3]};
        f32x4 a3 = {accS[ob + 4], accS[ob + 5], accS[ob + 6], accS[ob + 7]};
        ex[lane] = a0; ex[64 + lane] = a1; ex[128 + lane] = a2; ex[192 + lane] = a3;
    }
    wg_barrier();
    {
        LAS f32x4* ei = (LAS f32x4*)(sm + ((w & 3) * 2 + (1 - kh)) * 4096);
        f32x4 b0 = ei[lane], b1 = ei[64 + lane], b2 = ei[128 + lane], b3 = ei[192 + lane];
        const int kb = 8 * kh;
        const int token = 64 * r64 + 32 * ttl + (lane & 31);
#pragma unroll
        for (int g2 = 0; g2 < 2; ++g2) {
            int col = 64 * c64 + 32 * ct + 8 * (2 * kh + g2) + 4 * h;
            size_t off = ((size_t)token * 512 + col) * 2;
            uint2 xv = *(const uint2*)(x + off);
            float xs[4] = {__uint_as_float(xv.x << 16), __uint_as_float(xv.x & 0xffff0000u), __uint_as_float(xv.y << 16), __uint_as_float(xv.y & 0xffff0000u)};
            f32x4 bf = g2 ? b1 : b0, bs = g2 ? b3 : b2;
            float r[4];
#pragma unroll
            for (int i = 0; i < 4; ++i) {
                float fv = accF[kb + 4 * g2 + i] + bf[i];
                float sv = accS[kb + 4 * g2 + i] + bs[i];
                float e = __builtin_amdgcn_exp2f(fv * -1.4426950408889634f);
                float gg = fv * __builtin_amdgcn_rcpf(1.f + e);
                r[i] = xs[i] + sv * gg;
            }
            uint2 ov = {pack_bf16(r[0], r[1]), pack_bf16(r[2], r[3])};
            *(uint2*)(out + off) = ov;
        }
        (void)kb;
    }
}

extern "C" void kernel_launch(void* const* d_in, const int* in_sizes, int n_in, void* d_out, int out_size, void* d_ws, size_t ws_size,
                              hipStream_t stream) {
    const char* x = (const char*)d_in[0];
    const char* wqkv = (const char*)d_in[1];
    const char* wo = (const char*)d_in[2];
    const char* w1 = (const char*)d_in[4];
    const char* w3 = (const char*)d_in[5];
    const char* w2 = (const char*)d_in[6];
    char* ws = (char*)d_ws;
    char* Qf = ws;
    char* Kf = ws + (2u << 20);
    char* Vf = ws + (4u << 20);
    char* actf = ws + (6u << 20);
    hipFuncSetAttribute((const void*)k1_qkv_ffn, hipFuncAttributeMaxDynamicSharedMemorySize, K1_LDS);
    hipFuncSetAttribute((const void*)k2_attn, hipFuncAttributeMaxDynamicSharedMemorySize, K2_LDS);
    hipFuncSetAttribute((const void*)k3_out, hipFuncAttributeMaxDynamicSharedMemorySize, K3_LDS);
    k1_qkv_ffn<<<256, 512, K1_LDS, stream>>>(x, wqkv, w1, w3, Qf, Kf, Vf, actf);
    k2_attn<<<256, 512, K2_LDS, stream>>>(Qf, Kf, Vf, actf);
    k3_out<<<256, 512, K3_LDS, stream>>>(actf, w2, wo, x, (char*)d_out);
}
```

```cpp
#include <hip/hip_runtime.h>
#include <hip/hip_bf16.h>
#include <stdint.h>

typedef __attribute__((ext_vector_type(8))) short s16x8;
typedef __attribute__((ext_vector_type(4))) short s16x4;
typedef __attribute__((ext_vector_type(16))) float f32x16;
typedef __attribute__((ext_vector_type(4))) float f32x4;
typedef __attribute__((ext_vector_type(4))) unsigned u32x4;
typedef __attribute__((ext_vector_type(2))) __bf16 bf16x2_t;
typedef __attribute__((ext_vector_type(8))) __bf16 bf16x8_t;
typedef __attribute__((ext_vector_type(2))) float f32x2;
typedef __attribute__((ext_vector_type(4))) int rsrc_t;
#define LAS __attribute__((address_space(3)))
#define DEVINL __device__ __forceinline__
#define SBAR() __builtin_amdgcn_sched_barrier(0)
#define CBAR() asm volatile("" ::: "memory")

template <int I> struct IC { static constexpr int value = I; constexpr operator int() const { return I; } };
template <int B, int E, class F> DEVINL void static_for(F&& f) { if constexpr (B < E) { f(IC<B>{}); static_for<B + 1, E>(f); } }

template <int N> DEVINL void wait_vm() { asm volatile("s_waitcnt vmcnt(%0)" ::"n"(N) : "memory"); }
DEVINL void raw_barrier() { asm volatile("s_barrier" ::: "memory"); }
DEVINL void wg_barrier() { asm volatile("s_waitcnt lgkmcnt(0)\n\ts_barrier" ::: "memory"); }

DEVINL rsrc_t mk_rsrc(const void* p, unsigned nbytes) {
    unsigned long long a = (unsigned long long)p;
    rsrc_t r = {(int)(unsigned)a, (int)(unsigned)(a >> 32), (int)nbytes, 0x00020000};
    return r;
}
template <bool SC1> DEVINL void blds16(rsrc_t r, LAS void* ldst, int voff, int soff) {
    if constexpr (SC1)
        asm volatile("s_mov_b32 m0, %0\n\ts_nop 0\n\tbuffer_load_dwordx4 %1, %2, %3 offen sc1 lds"
                     : : "s"(__builtin_amdgcn_readfirstlane((unsigned)(size_t)ldst)), "v"(voff), "s"(r), "s"(__builtin_amdgcn_readfirstlane(soff)) : "memory");
    else
        asm volatile("s_mov_b32 m0, %0\n\ts_nop 0\n\tbuffer_load_dwordx4 %1, %2, %3 offen lds"
                     : : "s"(__builtin_amdgcn_readfirstlane((unsigned)(size_t)ldst)), "v"(voff), "s"(r), "s"(__builtin_amdgcn_readfirstlane(soff)) : "memory");
}
DEVINL f32x16 mfma32(s16x8 a, s16x8 b, f32x16 c) {
    return __builtin_amdgcn_mfma_f32_32x32x16_bf16(__builtin_bit_cast(bf16x8_t, a), __builtin_bit_cast(bf16x8_t, b), c, 0, 0, 0);
}
DEVINL s16x4 tr_read(const LAS char* p) { return __builtin_amdgcn_ds_read_tr16_b64_v4i16((LAS s16x4*)p); }
DEVINL s16x8 wfrag(const LAS char* p0, const LAS char* p1) {
    s16x4 a = tr_read(p0), b = tr_read(p1);
    return __builtin_shufflevector(a, b, 0, 1, 2, 3, 4, 5, 6, 7);
}
DEVINL unsigned pack_bf16(float lo, float hi) {
    f32x2 v = {lo, hi};
    return __builtin_bit_cast(unsigned, __builtin_convertvector(v, bf16x2_t));
}
DEVINL float dot2_acc(unsigned a, unsigned b, float c) {
    return __builtin_amdgcn_fdot2_f32_bf16(__builtin_bit_cast(bf16x2_t, a), __builtin_bit_cast(bf16x2_t, b), c, false);
}
#define ONES2 0x3F803F80u
DEVINL void store_frag16_sc1(__amdgpu_buffer_rsrc_t r, int voff, int soff, const float* v) {
    u32x4 o = {pack_bf16(v[0], v[1]), pack_bf16(v[2], v[3]), pack_bf16(v[4], v[5]), pack_bf16(v[6], v[7])};
    __builtin_amdgcn_raw_buffer_store_b128(o, r, voff, soff, 16);
}

static constexpr int S = 2048, D = 512, F = 1376;
static constexpr int NTT = 64;
static constexpr int NCH = 118;
static constexpr unsigned WS_KVF = 4096, WS_HF = 4096 + 16384, WS_OF = 4096 + 32768, WS_ARR = 4096 + 49152;
static constexpr unsigned WS_Q = 1u << 20, WS_K = 3u << 20, WS_V = 5u << 20, WS_ACT = 7u << 20;

static constexpr int SLOT = 16384;
static constexpr int SCR_CSP = 8 * SLOT;
static constexpr int SCR_AB = 8 * SLOT + 2048;
static constexpr int SCR_WV = 8 * SLOT + 3072;
static constexpr int SCR_TAG = 8 * SLOT + 3072 + 2048;
static constexpr int LDS_BYTES = 8 * SLOT + 3072 + 2048 + 16;

struct Ctx {
    rsrc_t rx, rq, rf1, rf3;
    rsrc_t rK, rV, rA, rw2, rwo;
    __amdgpu_buffer_rsrc_t sQ, sK, sV, sA;
    const char* x; char* out; char* ws;
    unsigned tag;
    int rb, hp, pair0;
    int w, rg, lane;
    int voff_x, voff_qkv, voff_ffn;
};
#define TSTAMP(i) do {} while (0)

DEVINL unsigned* flag_ptr(const Ctx& c, unsigned base, int idx) { return (unsigned*)(c.ws + base + idx * 64); }
DEVINL void flag_set(const Ctx& c, unsigned base, int idx) { __hip_atomic_store(flag_ptr(c, base, idx), c.tag, __ATOMIC_RELAXED, __HIP_MEMORY_SCOPE_AGENT); }
DEVINL void flags_wait(const Ctx& c, unsigned base, int idx0, int stride, int n) {
    unsigned* p = flag_ptr(c, base, idx0 + (c.lane < n ? c.lane : 0) * stride);
    for (int it = 0; it < (1 << 22); ++it) {
        unsigned v = __hip_atomic_load(p, __ATOMIC_RELAXED, __HIP_MEMORY_SCOPE_AGENT);
        if (__all(v == c.tag)) break;
        __builtin_amdgcn_s_sleep(1);
    }
    CBAR();
}

template <int CH, int NP> struct G1 {
    static constexpr int NR = (NP + 1) / 2;
    static constexpr int NITEMS = 16 + 8 * NR;
    static constexpr int issuer(int i) { return i < 24 ? 0 : 1; }
    static constexpr int cnt(int i) {
        if (i >= NITEMS || issuer(i) != CH) return 0;
        if (i < 16) return (i & 1) ? 3 : 4;
        return (2 * ((i - 16) >> 3) + 1 < NP) ? 4 : 2;
    }
    static constexpr int younger(int first, int last) { int n = 0; for (int i = first; i <= last; ++i) n += cnt(i); return n; }
};

DEVINL void g1_issue_x(const Ctx& c, LAS char* sm, int s, int slot) {
#pragma unroll
    for (int m = 0; m < 4; ++m) {
        int id = c.rg + 4 * m;
        blds16<false>(c.rx, sm + slot * SLOT + id * 1024, c.voff_x, (c.rb * 128 + 8 * id) * 1024 + s * 128);
    }
}
DEVINL void g1_issue_wqkv(const Ctx& c, LAS char* sm, int s, int slot) {
#pragma unroll
    for (int m = 0; m < 3; ++m)
        blds16<false>(c.rq, sm + slot * SLOT + (4 * m + c.rg) * 1024, c.voff_qkv, (64 * s + 16 * c.rg) * 3072 + (m * 512 + c.hp * 32) * 2);
}
template <int NP> DEVINL void g1_issue_ffn(const Ctx& c, LAS char* sm, int round, int j) {
#pragma unroll
    for (int m = 0; m < 4; ++m) {
        if (2 * round + (m >> 1) < NP)
            blds16<false>((m & 1) ? c.rf3 : c.rf1, sm + j * SLOT + (4 * m + c.rg) * 1024, c.voff_ffn, (64 * j + 16 * c.rg) * 2752 + (c.pair0 + 2 * round + (m >> 1)) * 64);
    }
}
template <int CH, int NP, int IT> DEVINL void g1_issue_item(const Ctx& c, LAS char* sm) {
    if constexpr (G1<CH, NP>::issuer(IT) != CH) {
    } else if constexpr (IT < 16) {
        if constexpr ((IT & 1) == 0) g1_issue_x(c, sm, IT >> 1, IT & 7);
        else g1_issue_wqkv(c, sm, IT >> 1, IT & 7);
    } else if constexpr (IT < G1<CH, NP>::NITEMS) {
        g1_issue_ffn<NP>(c, sm, (IT - 16) >> 3, (IT - 16) & 7);
    }
}
DEVINL void g1_load_cs(LAS char* sm, int wvoff, int csp_tile, int lane, f32x4 (&c4)[4]) {
    LAS float* csp = (LAS float*)(sm + SCR_CSP) + csp_tile * 128;
    LAS float* csf = (LAS float*)(sm + wvoff);
    int n = lane & 31;
    float v = (csp[n] + csp[32 + n]) + (csp[64 + n] + csp[96 + n]);
    if (lane < 32) csf[n] = v;
    CBAR();
    int h = lane >> 5;
#pragma unroll
    for (int g = 0; g < 4; ++g) c4[g] = *(LAS f32x4*)(csf + 8 * g + 4 * h);
}

template <int CH, int NP>
DEVINL void phase_ab(Ctx& c, LAS char* sm) {
    using G = G1<CH, NP>;
    const int lane = c.lane, rg = c.rg, h = lane >> 5;
    const int wl = (8 * h + ((lane & 15) >> 2)) * 64 + ((lane >> 4) & 1) * 32 + (lane & 3) * 8;
    const int xr = 32 * rg + (lane & 31);
    const int xm = (xr >> 1) & 7;
    const int xa0 = xr * 128 + ((h ^ (xm & 1)) << 4) + ((xm >> 1) << 5);
    const int wvoff = SCR_WV + c.w * 256;

    s16x8 xf[32];
    float s1 = 0.f, s2 = 0.f;
    f32x16 accA = {0}, accB = {0};
    float csA = 0.f, csB = 0.f;
    s16x8 a0, a1, b0, b1;

    static_for<0, 8>([&](auto SI) {
        constexpr int s = SI;
        constexpr int need = s < 7 ? 2 * s + 3 : 16;
        constexpr int issued = s == 0 ? 7 : 2 * s + 5;
#define F1(k) do {} while (0)
        F1(0);
        wait_vm<G::younger(need + 1, issued)>();
        F1(1);
        if constexpr (s == 0) wg_barrier(); else raw_barrier();
        F1(2);
        if constexpr (s == 0) c.tag = *(volatile LAS unsigned*)(sm + SCR_TAG);
        if constexpr (s == 0) TSTAMP(1);
        if constexpr (s >= 1 && CH == 0) { g1_issue_item<CH, NP, 2 * s + 6>(c, sm); g1_issue_item<CH, NP, 2 * s + 7>(c, sm); }
        const LAS char* xs = sm + ((2 * s) & 7) * SLOT;
        const LAS char* wa = sm + ((2 * s + 1) & 7) * SLOT + (CH == 0 ? 2 * 4096 : 0) + wl;
        const LAS char* wb = wa + 4096;
        const LAS char* nxs = sm + ((2 * s + 2) & 7) * SLOT;
        const LAS char* nwa = sm + ((2 * s + 3) & 7) * SLOT + (CH == 0 ? 2 * 4096 : 0) + wl;
        const LAS char* nwb = nwa + 4096;
        if constexpr (s == 0) {
            xf[0] = *(const LAS s16x8*)(xs + xa0);
            xf[1] = *(const LAS s16x8*)(xs + (xa0 ^ 32));
            a0 = wfrag(wa, wa + 256); a1 = wfrag(wa + 1024, wa + 1024 + 256);
            if constexpr (CH == 1) { b0 = wfrag(wb, wb + 256); b1 = wfrag(wb + 1024, wb + 1024 + 256); }
        }
        if constexpr (CH == 1) {
            auto stats = [&](s16x8 v) {
                u32x4 u = __builtin_bit_cast(u32x4, v);
#pragma unroll
                for (int q = 0; q < 4; ++q) { s1 = dot2_acc(u[q], ONES2, s1); s2 = dot2_acc(u[q], u[q], s2); }
            };
            SBAR(); F1(3);
            accA = mfma32(a0, xf[4 * s + 0], accA);
            stats(xf[4 * s + 0]);
            xf[4 * s + 2] = *(const LAS s16x8*)(xs + (xa0 ^ 64));
            s16x8 a2 = wfrag(wa + 2048, wa + 2048 + 256);
            SBAR(); F1(4);
            accB = mfma32(b0, xf[4 * s + 0], accB);
            s16x8 b2 = wfrag(wb + 2048, wb + 2048 + 256);
            s16x8 ca = wfrag(wa + rg * 1024, wa + rg * 1024 + 256);
            SBAR(); F1(5);
            accA = mfma32(a1, xf[4 * s + 1], accA);
            stats(xf[4 * s + 1]);
            xf[4 * s + 3] = *(const LAS s16x8*)(xs + (xa0 ^ 96));
            s16x8 a3 = wfrag(wa + 3072, wa + 3072 + 256);
            SBAR(); F1(6);
            accB = mfma32(b1, xf[4 * s + 1], accB);
            s16x8 b3 = wfrag(wb + 3072, wb + 3072 + 256);
            s16x8 cb = wfrag(wb + rg * 1024, wb + rg * 1024 + 256);
            SBAR(); F1(7);
            accA = mfma32(a2, xf[4 * s + 2], accA);
            stats(xf[4 * s + 2]);
            if constexpr (s < 7) { xf[4 * s + 4] = *(const LAS s16x8*)(nxs + xa0); a0 = wfrag(nwa, nwa + 256); }
            SBAR(); F1(8);
            accB = mfma32(b2, xf[4 * s + 2], accB);
            if constexpr (s < 7) { b0 = wfrag(nwb, nwb + 256); }
            u32x4 cau = __builtin_bit_cast(u32x4, ca);
#pragma unroll
            for (int q = 0; q < 4; ++q) csA = dot2_acc(cau[q], ONES2, csA);
            SBAR(); F1(9);
            accA = mfma32(a3, xf[4 * s + 3], accA);
            stats(xf[4 * s + 3]);
            if constexpr (s < 7) { xf[4 * s + 5] = *(const LAS s16x8*)(nxs + (xa0 ^ 32)); a1 = wfrag(nwa + 1024, nwa + 1024 + 256); }
            SBAR(); F1(10);
            accB = mfma32(b3, xf[4 * s + 3], accB);
            if constexpr (s < 7) { b1 = wfrag(nwb + 1024, nwb + 1024 + 256); }
            u32x4 cbu = __builtin_bit_cast(u32x4, cb);
#pragma unroll
            for (int q = 0; q < 4; ++q) csB = dot2_acc(cbu[q], ONES2, csB);
            SBAR(); F1(11);
            if constexpr (s >= 1) { g1_issue_item<CH, NP, 2 * s + 6>(c, sm); g1_issue_item<CH, NP, 2 * s + 7>(c, sm); }
        } else {
            SBAR(); F1(3);
            accA = mfma32(xf[4 * s + 0], a0, accA);
            xf[4 * s + 2] = *(const LAS s16x8*)(xs + (xa0 ^ 64));
            s16x8 a2 = wfrag(wa + 2048, wa + 2048 + 256);
            s16x8 ca = wfrag(wa + rg * 1024, wa + rg * 1024 + 256);
            SBAR(); F1(4);
            accA = mfma32(xf[4 * s + 1], a1, accA);
            xf[4 * s + 3] = *(const LAS s16x8*)(xs + (xa0 ^ 96));
            s16x8 a3 = wfrag(wa + 3072, wa + 3072 + 256);
            SBAR(); F1(5);
            accA = mfma32(xf[4 * s + 2], a2, accA);
            if constexpr (s < 7) { xf[4 * s + 4] = *(const LAS s16x8*)(nxs + xa0); a0 = wfrag(nwa, nwa + 256); }
            u32x4 cau = __builtin_bit_cast(u32x4, ca);
#pragma unroll
            for (int q = 0; q < 4; ++q) csA = dot2_acc(cau[q], ONES2, csA);
            SBAR(); F1(6);
            accA = mfma32(xf[4 * s + 3], a3, accA);
            if constexpr (s < 7) { xf[4 * s + 5] = *(const LAS s16x8*)(nxs + (xa0 ^ 32)); a1 = wfrag(nwa + 1024, nwa + 1024 + 256); }
            SBAR(); F1(7);
        }
    });
    TSTAMP(2);

    LAS float* csp = (LAS float*)(sm + SCR_CSP);
    LAS float* AB = (LAS float*)(sm + SCR_AB) + rg * 64;
    {
        float pa = csA + __shfl_xor(csA, 32);
        if constexpr (CH == 1) {
            s1 += __shfl_xor(s1, 32);
            s2 += __shfl_xor(s2, 32);
            float mean = s1 * (1.f / 512.f);
            float var = fmaxf(s2 * (1.f / 512.f) - mean * mean, 0.f);
            float rstd = 1.0f / sqrtf(var + 1e-5f);
            float pb = csB + __shfl_xor(csB, 32);
            if (lane < 32) { AB[lane] = rstd; AB[32 + lane] = -mean * rstd; csp[0 * 128 + rg * 32 + lane] = pa; csp[1 * 128 + rg * 32 + lane] = pb; }
        } else {
            if (lane < 32) csp[2 * 128 + rg * 32 + lane] = pa;
        }
    }
    const int tt = 4 * c.rb + rg;
    wait_vm<G::younger(18, 21)>();
    wg_barrier();
    {
        if constexpr (CH == 0) {
            LAS float* cspv = csp + 2 * 128;
            int n = lane & 31;
            float csV = (cspv[n] + cspv[32 + n]) + (cspv[64 + n] + cspv[96 + n]);
            float v[16];
#pragma unroll
            for (int g = 0; g < 4; ++g) {
                f32x4 A4 = *(LAS f32x4*)(AB + 8 * g + 4 * h);
                f32x4 B4 = *(LAS f32x4*)(AB + 32 + 8 * g + 4 * h);
#pragma unroll
                for (int i = 0; i < 4; ++i) v[4 * g + i] = fmaf(accA[4 * g + i], A4[i], csV * B4[i]);
            }
            int soff = c.hp * 2 * NTT * 1024 + tt * 1024;
            int voff = (n >> 4) * NTT * 1024 + (h * 16 + (n & 15)) * 16;
            store_frag16_sc1(c.sV, voff, soff, v);
            store_frag16_sc1(c.sV, voff, soff + 512, v + 8);
        } else {
            const float ra = AB[lane & 31], rbm = AB[32 + (lane & 31)];
            static_for<0, 2>([&](auto TI) {
                constexpr int t = TI;
                f32x4 c4[4];
                g1_load_cs(sm, wvoff + 128 * t, t, lane, c4);
                const f32x16& acc = t == 0 ? accA : accB;
                float v[16];
#pragma unroll
                for (int g = 0; g < 4; ++g)
#pragma unroll
                    for (int i = 0; i < 4; ++i) v[4 * g + i] = fmaf(acc[4 * g + i], ra, rbm * c4[g][i]);
                int soff = (2 * c.hp * NTT + tt) * 1024;
                store_frag16_sc1(t == 0 ? c.sQ : c.sK, lane * 16, soff, v);
                store_frag16_sc1(t == 0 ? c.sQ : c.sK, lane * 16, soff + NTT * 1024, v + 8);
            });
        }
    }
    g1_issue_item<CH, NP, 22>(c, sm);
    g1_issue_item<CH, NP, 23>(c, sm);
    TSTAMP(3);
    const float ra = AB[lane & 31], rbm = AB[32 + (lane & 31)];

    s16x8 fG0, fU0, fG1, fU1;
    if constexpr (CH < NP) {
        const LAS char* p = sm + (CH * 2) * 4096 + wl;
        fG0 = wfrag(p, p + 256); fU0 = wfrag(p + 4096, p + 4096 + 256);
        fG1 = wfrag(p + 1024, p + 1024 + 256); fU1 = wfrag(p + 4096 + 1024, p + 4096 + 1024 + 256);
    }
    static_for<0, G::NR>([&](auto RI) {
        constexpr int r = RI;
        constexpr bool active = (2 * r + CH) < NP;
        f32x16 accG = {0}, accU = {0};
        float csG = 0.f, csU = 0.f;
        static_for<0, 8>([&](auto JI) {
            constexpr int j = JI;
            constexpr int i = 16 + 8 * r + j;
#define FS(k) do {} while (0)
            FS(0);
            if constexpr (i > 16) {
                wait_vm<G::younger(i + 2, i + 6)>();
                FS(1);
                raw_barrier();
                FS(2);
                if constexpr (CH == 0) g1_issue_item<CH, NP, i + 7>(c, sm);
            }
            FS(3);
            if constexpr (i == 21) {
                if (threadIdx.x == 0) { flag_set(c, WS_KVF, c.rb * 16 + c.hp); flag_set(c, WS_ARR, blockIdx.x); }
            }
            if constexpr (active) {
                const LAS char* wg_ = sm + j * SLOT + (CH * 2) * 4096 + wl;
                const LAS char* wu_ = wg_ + 4096;
                const LAS char* ng_ = sm + ((j + 1) & 7) * SLOT + (CH * 2) * 4096 + wl;
                const LAS char* nu_ = ng_ + 4096;
                SBAR();
                accG = mfma32(fG0, xf[4 * j + 0], accG);
                s16x8 g2 = wfrag(wg_ + 2048, wg_ + 2048 + 256);
                s16x8 cgf = wfrag(wg_ + rg * 1024, wg_ + rg * 1024 + 256);
                SBAR(); FS(4);
                accU = mfma32(fU0, xf[4 * j + 0], accU);
                s16x8 u2 = wfrag(wu_ + 2048, wu_ + 2048 + 256);
                s16x8 cuf = wfrag(wu_ + rg * 1024, wu_ + rg * 1024 + 256);
                SBAR(); FS(5);
                accG = mfma32(fG1, xf[4 * j + 1], accG);
                s16x8 g3 = wfrag(wg_ + 3072, wg_ + 3072 + 256);
                SBAR(); FS(6);
                accU = mfma32(fU1, xf[4 * j + 1], accU);
                s16x8 u3 = wfrag(wu_ + 3072, wu_ + 3072 + 256);
                SBAR(); FS(7);
                accG = mfma32(g2, xf[4 * j + 2], accG);
                fG0 = wfrag(ng_, ng_ + 256);
                SBAR(); FS(8);
                accU = mfma32(u2, xf[4 * j + 2], accU);
                fU0 = wfrag(nu_, nu_ + 256);
                u32x4 wgq = __builtin_bit_cast(u32x4, cgf), wuq = __builtin_bit_cast(u32x4, cuf);
                csG = dot2_acc(wgq[0], ONES2, csG); csU = dot2_acc(wuq[0], ONES2, csU);
                csG = dot2_acc(wgq[1], ONES2, csG); csU = dot2_acc(wuq[1], ONES2, csU);
                SBAR(); FS(9);
                accG = mfma32(g3, xf[4 * j + 3], accG);
                fG1 = wfrag(ng_ + 1024, ng_ + 1024 + 256);
                csG = dot2_acc(wgq[2], ONES2, csG); csU = dot2_acc(wuq[2], ONES2, csU);
                SBAR(); FS(10);
                accU = mfma32(u3, xf[4 * j + 3], accU);
                fU1 = wfrag(nu_ + 1024, nu_ + 1024 + 256);
                csG = dot2_acc(wgq[3], ONES2, csG); csU = dot2_acc(wuq[3], ONES2, csU);
                SBAR(); FS(11);
            }
            if constexpr (CH == 1 && i > 16) g1_issue_item<CH, NP, i + 7>(c, sm);
        });
        TSTAMP(4 + 2 * r);
        if constexpr (active) {
            float pa = csG + __shfl_xor(csG, 32);
            float pb = csU + __shfl_xor(csU, 32);
            if (lane < 32) { csp[(CH * 2) * 128 + rg * 32 + lane] = pa; csp[(CH * 2 + 1) * 128 + rg * 32 + lane] = pb; }
        }
        wg_barrier();
        if constexpr (active) {
            f32x4 cg4[4], cu4[4];
            g1_load_cs(sm, wvoff, CH * 2, lane, cg4);
            g1_load_cs(sm, wvoff + 128, CH * 2 + 1, lane, cu4);
            float v[16];
#pragma unroll
            for (int g = 0; g < 4; ++g)
#pragma unroll
                for (int i = 0; i < 4; ++i) {
                    float gv = fmaf(accG[4 * g + i], ra, rbm * cg4[g][i]);
                    float uv = fmaf(accU[4 * g + i], ra, rbm * cu4[g][i]);
                    float e = __builtin_amdgcn_exp2f(gv * -1.4426950408889634f);
                    float sg = gv * __builtin_amdgcn_rcpf(1.f + e);
                    v[4 * g + i] = sg * uv;
                }
            constexpr int prel = 2 * r + CH;
            int soff = (tt * NCH + 2 * (c.pair0 + prel)) * 1024;
            store_frag16_sc1(c.sA, lane * 16, soff, v);
            store_frag16_sc1(c.sA, lane * 16, soff + 1024, v + 8);
        }
        TSTAMP(5 + 2 * r);
    });
}

static constexpr int AT_K = 0, AT_V = 24576, AT_ONES = 49152;
DEVINL void phase_attn(const Ctx& c, LAS char* sm) {
    const int hp = c.hp, qb = c.rb, w = c.w, lane = c.lane;
    const int hl = w >> 2, qi = w & 3, head = 2 * hp + hl, qt = 4 * qb + qi;
    const int h = lane >> 5;
#pragma unroll
    for (int m = 0; m < 6; ++m) {
        int id = w + 8 * m;
        int arr = id / 24, hh = (id % 24) / 12, t = id % 12;
        int tt = 4 * qb - 8 + t; if (tt < 0) tt = 0;
        blds16<true>(arr ? c.rV : c.rK, sm + arr * 24576 + (hh * 12 + t) * 1024, lane * 16, ((2 * hp + hh) * NTT + tt) * 1024);
    }
    s16x8 qf;
    {
        const char* qp = c.ws + WS_Q + (size_t)(head * NTT + qt) * 1024 + lane * 16;
        asm volatile("global_load_dwordx4 %0, %1, off sc1" : "=v"(qf) : "v"(qp) : "memory");
    }
    {
        u32x4 ones = {ONES2, ONES2, ONES2, ONES2};
        *(LAS u32x4*)(sm + AT_ONES + threadIdx.x * 16) = ones;
        if (threadIdx.x < 256) *(LAS u32x4*)(sm + AT_ONES + 8192 + threadIdx.x * 16) = ones;
    }
#define FA(k) do {} while (0)
    FA(0);
    asm volatile("s_waitcnt vmcnt(0) lgkmcnt(0)\n\ts_barrier" : "+v"(qf) :: "memory");
    SBAR();
    FA(1);

    f32x16 s[9];
    const LAS char* kb = sm + AT_K + (hl * 12 + qi) * 1024 + lane * 16;
    static_for<0, 9>([&](auto TI) {
        constexpr int t = TI;
        s16x8 kf = *(const LAS s16x8*)(kb + t * 1024);
        f32x16 z = {0};
        s[t] = mfma32(kf, qf, z);
    });
    SBAR(); FA(2);
    const int ql = lane & 31;
    const float NEG = -__builtin_inff();
#pragma unroll
    for (int r = 0; r < 16; ++r) {
        int kl = (r & 3) + 8 * (r >> 2) + 4 * h;
        s[0][r] = (kl > ql) ? s[0][r] : NEG;
        s[8][r] = (kl <= ql) ? s[8][r] : NEG;
    }
    if (qt < 8) {
        static_for<0, 8>([&](auto TI) {
            constexpr int t = TI;
            if (qt - 8 + t < 0) {
#pragma unroll
                for (int r = 0; r < 16; ++r) s[t][r] = NEG;
            }
        });
    }
    float m = s[8][0];
    static_for<0, 9>([&](auto TI) {
        constexpr int t = TI;
#pragma unroll
        for (int r = 0; r < 16; r += 2) m = __builtin_fmaxf(m, __builtin_fmaxf(s[t][r], s[t][r + 1]));
    });
    m = fmaxf(m, __shfl_xor(m, 32));
    const float cs = 0.25f * 1.4426950408889634f;
    const float mc = -m * cs;
    SBAR(); FA(3);
    f32x16 o = {0};
    const LAS char* vb = sm + ((lane & 16) ? AT_ONES : (AT_V + hl * 12288)) + qi * 1024 + (h * 16 + (lane & 15)) * 16;
    static_for<0, 9>([&](auto TI) {
        constexpr int t = TI;
        float p[16];
#pragma unroll
        for (int r = 0; r < 16; ++r) p[r] = __builtin_amdgcn_exp2f(fmaf(s[t][r], cs, mc));
#pragma unroll
        for (int k2 = 0; k2 < 2; ++k2) {
            u32x4 pk = {pack_bf16(p[8 * k2 + 0], p[8 * k2 + 1]), pack_bf16(p[8 * k2 + 2], p[8 * k2 + 3]),
                        pack_bf16(p[8 * k2 + 4], p[8 * k2 + 5]), pack_bf16(p[8 * k2 + 6], p[8 * k2 + 7])};
            s16x8 vf = *(const LAS s16x8*)(vb + t * 1024 + k2 * 512);
            o = mfma32(vf, __builtin_bit_cast(s16x8, pk), o);
        }
    });
    SBAR(); FA(4);
    float inv = 1.f / o[8];
    float v[8];
#pragma unroll
    for (int r = 0; r < 8; ++r) v[r] = o[r] * inv;
    store_frag16_sc1(c.sA, lane * 16, (qt * NCH + 86 + head) * 1024, v);
}

DEVINL void g3_issue(const Ctx& c, LAS char* sm, int r64, int c64, int voff_w, int st) {
    const int q = c.rg, slot = st & 7;
    int chunk0 = st < 22 ? 4 * st : 86 + 4 * (st - 22);
#pragma unroll
    for (int m = 0; m < 2; ++m) blds16<true>(c.rA, sm + slot * SLOT + (4 * m + q) * 1024, c.lane * 16, ((2 * r64 + m) * NCH + chunk0 + q) * 1024);
#pragma unroll
    for (int m = 0; m < 2; ++m) {
        int k0 = (st < 22 ? 64 * st : 64 * (st - 22)) + 8 * (4 * m + q);
        if (st == 21 && m == 1) k0 = 64 * 21;
        blds16<false>(st < 22 ? c.rw2 : c.rwo, sm + slot * SLOT + 8192 + (4 * m + q) * 1024, voff_w, k0 * 1024 + c64 * 128);
    }
}
DEVINL void phase_out_loader(const Ctx& c, LAS char* sm, int r64, int c64) {
    const int lane = c.lane;
    const int voff_w = (lane >> 3) * 1024 + (((lane & 7) ^ ((((lane >> 3) >> 1) & 1) << 2)) << 4);
    static_for<0, 7>([&](auto SI) { g3_issue(c, sm, r64, c64, voff_w, SI); });
    static_for<0, 30>([&](auto SI) {
        constexpr int st = SI;
        constexpr int last_issued = st + 6 < 29 ? st + 6 : 29;
        constexpr int need = st + 1 < 29 ? st + 1 : 29;
        constexpr int NW = st == 16 ? 4 * (21 - need) : 4 * (last_issued - need);
        wait_vm<NW>();
        raw_barrier();
        if constexpr (st + 7 < 30) {
            if constexpr (st + 7 == 22) {
                if (c.w == 4) flags_wait(c, WS_OF, (r64 >> 1) * 16, 1, 16);
                if (c.w == 4) g3_issue(c, sm, r64, c64, voff_w, 22);
            } else if constexpr (st + 7 == 23) {
                if (c.w != 4) g3_issue(c, sm, r64, c64, voff_w, 22);
                g3_issue(c, sm, r64, c64, voff_w, 23);
            } else {
                g3_issue(c, sm, r64, c64, voff_w, st + 7);
            }
        }
    });
    raw_barrier();
}
DEVINL void phase_out_consumer(const Ctx& c, LAS char* sm, int r64, int c64) {
    const int w = c.w, lane = c.lane;
    const int ttl = w & 1, ct = (w >> 1) & 1, h = lane >> 5;
    const int q = (lane & 15) >> 2, p = lane & 3, grp = (lane >> 4) & 1;
    const int wl = 8192 + (4 * h + q) * 128 + (((ct * 4 + grp * 2 + (p >> 1)) ^ (((q >> 1) & 1) << 2)) << 4) + (p & 1) * 8;
    const int al = ttl * 4096 + lane * 16;
    f32x16 accF = {0}, accS = {0};
    s16x8 fa[4], fw[4];
    static_for<0, 30>([&](auto SI) {
        constexpr int st = SI;
        raw_barrier();
        const LAS char* sl = sm + (st & 7) * SLOT;
        const LAS char* nsl = sm + ((st + 1) & 7) * SLOT;
        if constexpr (st == 0) {
            static_for<0, 4>([&](auto KI) { constexpr int ks = KI; fa[ks] = *(const LAS s16x8*)(sl + al + ks * 1024); fw[ks] = wfrag(sl + wl + ks * 2048, sl + wl + ks * 2048 + 1024); });
        }
        static_for<0, 4>([&](auto KI) {
            constexpr int ks = KI;
            SBAR();
            if constexpr (!(st == 21 && ks >= 2)) { if constexpr (st < 22) accF = mfma32(fw[ks], fa[ks], accF); else accS = mfma32(fw[ks], fa[ks], accS); }
            if constexpr (st + 1 < 30) { fa[ks] = *(const LAS s16x8*)(nsl + al + ks * 1024); fw[ks] = wfrag(nsl + wl + ks * 2048, nsl + wl + ks * 2048 + 1024); }
        });
        SBAR();
    });
    raw_barrier();
    const int token = 64 * r64 + 32 * ttl + (lane & 31);
#pragma unroll
    for (int g = 0; g < 4; ++g) {
        int col = 64 * c64 + 32 * ct + 8 * g + 4 * h;
        size_t off = ((size_t)token * 512 + col) * 2;
        uint2 xv = *(const uint2*)(c.x + off);
        float xs[4] = {__uint_as_float(xv.x << 16), __uint_as_float(xv.x & 0xffff0000u), __uint_as_float(xv.y << 16), __uint_as_float(xv.y & 0xffff0000u)};
        float r[4];
#pragma unroll
        for (int i = 0; i < 4; ++i) {
            float fv = accF[4 * g + i], sv = accS[4 * g + i];
            float e = __builtin_amdgcn_exp2f(fv * -1.4426950408889634f);
            float gg = fv * __builtin_amdgcn_rcpf(1.f + e);
            r[i] = xs[i] + sv * gg;
        }
        uint2 ov = {pack_bf16(r[0], r[1]), pack_bf16(r[2], r[3])};
        *(uint2*)(c.out + off) = ov;
    }
}

#define PHASE_MASK 7
template <int PMASK>
__global__ __launch_bounds__(512, 2) void fused_kernel(const char* __restrict__ x, const char* __restrict__ wqkv, const char* __restrict__ wo,
                                                       const char* __restrict__ w1, const char* __restrict__ w3, const char* __restrict__ w2,
                                                       char* out, char* ws) {
    extern __shared__ __attribute__((aligned(16))) char smem[];
    LAS char* sm = (LAS char*)smem;
    Ctx c;
    const int b = blockIdx.x, xcd = b & 7, j = b >> 3;
    c.rb = (xcd >> 2) * 8 + (j & 7);
    const int cg = (xcd & 3) * 4 + (j >> 3);
    c.hp = cg;
    const int np = cg < 11 ? 3 : 2;
    c.pair0 = cg < 11 ? 3 * cg : 33 + 2 * (cg - 11);
    c.w = __builtin_amdgcn_readfirstlane(threadIdx.x >> 6);
    c.rg = c.w & 3;
    c.lane = threadIdx.x & 63;
    c.voff_qkv = (c.lane >> 2) * 3072 + (c.lane & 3) * 16;
    c.voff_ffn = (c.lane >> 2) * 2752 + (c.lane & 3) * 16;
    c.voff_x = (c.lane >> 3) * 1024 + (((c.lane & 7) ^ ((4 * (c.w & 1) + (c.lane >> 4)) & 7)) << 4);
    c.rx = mk_rsrc(x, S * D * 2);
    c.rq = mk_rsrc(wqkv, D * 3 * D * 2);
    c.rf1 = mk_rsrc(w1, D * F * 2); c.rf3 = mk_rsrc(w3, D * F * 2);
    c.rK = mk_rsrc(ws + WS_K, 2u << 20); c.rV = mk_rsrc(ws + WS_V, 2u << 20); c.rA = mk_rsrc(ws + WS_ACT, NTT * NCH * 1024);
    c.rw2 = mk_rsrc(w2, F * D * 2); c.rwo = mk_rsrc(wo, D * D * 2);
    c.sQ = __builtin_amdgcn_make_buffer_rsrc(ws + WS_Q, 0, 2u << 20, 0x00020000);
    c.sK = __builtin_amdgcn_make_buffer_rsrc(ws + WS_K, 0, 2u << 20, 0x00020000);
    c.sV = __builtin_amdgcn_make_buffer_rsrc(ws + WS_V, 0, 2u << 20, 0x00020000);
    c.sA = __builtin_amdgcn_make_buffer_rsrc(ws + WS_ACT, 0, NTT * NCH * 1024, 0x00020000);
    c.x = x; c.out = out; c.ws = ws;
    TSTAMP(0);
    if (c.w == 4) {
        unsigned e = __hip_atomic_load((unsigned*)ws, __ATOMIC_RELAXED, __HIP_MEMORY_SCOPE_AGENT);
        if (c.lane == 0) *(volatile LAS unsigned*)(sm + SCR_TAG) = e + 1u;
    }
    if constexpr (!(PMASK & 1)) { wg_barrier(); c.tag = *(volatile LAS unsigned*)(sm + SCR_TAG); }
    if constexpr (PMASK & 1) {
        if (c.w < 4) { static_for<0, 8>([&](auto I) { if (np == 3) g1_issue_item<0, 3, I>(c, sm); else g1_issue_item<0, 2, I>(c, sm); }); }
        else { static_for<0, 8>([&](auto I) { if (np == 3) g1_issue_item<1, 3, I>(c, sm); else g1_issue_item<1, 2, I>(c, sm); }); }
    }
    if constexpr (PMASK & 1) {
        if (c.w < 4) { if (np == 3) phase_ab<0, 3>(c, sm); else phase_ab<0, 2>(c, sm); }
        else { if (np == 3) phase_ab<1, 3>(c, sm); else phase_ab<1, 2>(c, sm); }
        wait_vm<0>();
        raw_barrier();
        if (threadIdx.x == 0) flag_set(c, WS_HF, c.rb * 16 + c.hp);
    }
    TSTAMP(8);
    if constexpr (PMASK & 2) {
        if (c.w == 0) {
            int r0 = c.rb >= 2 ? c.rb - 2 : 0;
            flags_wait(c, WS_KVF, r0 * 16 + c.hp, 16, c.rb - r0 + 1);
        }
        raw_barrier();
        TSTAMP(9);
        phase_attn(c, sm);
        wait_vm<0>();
        raw_barrier();
        if (threadIdx.x == 0) flag_set(c, WS_OF, c.rb * 16 + c.hp);
    }
    TSTAMP(10);
    if constexpr (PMASK & 4) {
        const int r64 = 4 * xcd + (j >> 3), c64 = j & 7;
        if (c.w == 0) flags_wait(c, WS_HF, (r64 >> 1) * 16, 1, 16);
        raw_barrier();
        TSTAMP(11);
        if (c.w < 4) phase_out_consumer(c, sm, r64, c64); else phase_out_loader(c, sm, r64, c64);
        TSTAMP(12);
        if (b == 0 && c.w == 0) {
            for (int g = 0; g < 4; ++g) flags_wait(c, WS_ARR, g * 64, 1, 64);
            if (c.lane == 0) __hip_atomic_store((unsigned*)ws, c.tag, __ATOMIC_RELAXED, __HIP_MEMORY_SCOPE_AGENT);
        }
    }
}

extern "C" void kernel_launch(void* const* d_in, const int* in_sizes, int n_in, void* d_out, int out_size, void* d_ws, size_t ws_size,
                              hipStream_t stream) {
    const char* x = (const char*)d_in[0];
    const char* wqkv = (const char*)d_in[1];
    const char* wo = (const char*)d_in[2];
    const char* w1 = (const char*)d_in[4];
    const char* w3 = (const char*)d_in[5];
    const char* w2 = (const char*)d_in[6];
    hipFuncSetAttribute((const void*)fused_kernel<7>, hipFuncAttributeMaxDynamicSharedMemorySize, LDS_BYTES);
    fused_kernel<7><<<256, 512, LDS_BYTES, stream>>>(x, wqkv, wo, w1, w3, w2, (char*)d_out, (char*)d_ws);
}
```

```cpp
#include <hip/hip_runtime.h>
#include <hip/hip_bf16.h>
#include <stdint.h>

typedef __attribute__((ext_vector_type(8))) short s16x8;
typedef __attribute__((ext_vector_type(4))) short s16x4;
typedef __attribute__((ext_vector_type(16))) float f32x16;
typedef __attribute__((ext_vector_type(4))) float f32x4;
typedef __attribute__((ext_vector_type(4))) unsigned u32x4;
typedef __attribute__((ext_vector_type(2))) __bf16 bf16x2_t;
typedef __attribute__((ext_vector_type(8))) __bf16 bf16x8_t;
typedef __attribute__((ext_vector_type(2))) float f32x2;
typedef __attribute__((ext_vector_type(4))) int rsrc_t;
#define LAS __attribute__((address_space(3)))
#define DEVINL __device__ __forceinline__
#define SBAR() __builtin_amdgcn_sched_barrier(0)
#define CBAR() asm volatile("" ::: "memory")

template <int I> struct IC { static constexpr int value = I; constexpr operator int() const { return I; } };
template <int B, int E, class F> DEVINL void static_for(F&& f) { if constexpr (B < E) { f(IC<B>{}); static_for<B + 1, E>(f); } }

template <int N> DEVINL void wait_vm() { asm volatile("s_waitcnt vmcnt(%0)" ::"n"(N) : "memory"); }
DEVINL void raw_barrier() { asm volatile("s_barrier" ::: "memory"); }
DEVINL void wg_barrier() { asm volatile("s_waitcnt lgkmcnt(0)\n\ts_barrier" ::: "memory"); }

DEVINL rsrc_t mk_rsrc(const void* p, unsigned nbytes) {
    unsigned long long a = (unsigned long long)p;
    rsrc_t r = {(int)(unsigned)a, (int)(unsigned)(a >> 32), (int)nbytes, 0x00020000};
    return r;
}
template <bool SC1> DEVINL void blds16(rsrc_t r, LAS void* ldst, int voff, int soff) {
    if constexpr (SC1)
        asm volatile("s_mov_b32 m0, %0\n\ts_nop 0\n\tbuffer_load_dwordx4 %1, %2, %3 offen sc1 lds"
                     : : "s"(__builtin_amdgcn_readfirstlane((unsigned)(size_t)ldst)), "v"(voff), "s"(r), "s"(__builtin_amdgcn_readfirstlane(soff)) : "memory");
    else
        asm volatile("s_mov_b32 m0, %0\n\ts_nop 0\n\tbuffer_load_dwordx4 %1, %2, %3 offen lds"
                     : : "s"(__builtin_amdgcn_readfirstlane((unsigned)(size_t)ldst)), "v"(voff), "s"(r), "s"(__builtin_amdgcn_readfirstlane(soff)) : "memory");
}
DEVINL f32x16 mfma32(s16x8 a, s16x8 b, f32x16 c) {
    return __builtin_amdgcn_mfma_f32_32x32x16_bf16(__builtin_bit_cast(bf16x8_t, a), __builtin_bit_cast(bf16x8_t, b), c, 0, 0, 0);
}
DEVINL s16x4 tr_read(const LAS char* p) { return __builtin_amdgcn_ds_read_tr16_b64_v4i16((LAS s16x4*)p); }
DEVINL s16x8 wfrag(const LAS char* p0, const LAS char* p1) {
    s16x4 a = tr_read(p0), b = tr_read(p1);
    return __builtin_shufflevector(a, b, 0, 1, 2, 3, 4, 5, 6, 7);
}
DEVINL unsigned pack_bf16(float lo, float hi) {
    f32x2 v = {lo, hi};
    return __builtin_bit_cast(unsigned, __builtin_convertvector(v, bf16x2_t));
}
DEVINL float dot2_acc(unsigned a, unsigned b, float c) {
    return __builtin_amdgcn_fdot2_f32_bf16(__builtin_bit_cast(bf16x2_t, a), __builtin_bit_cast(bf16x2_t, b), c, false);
}
#define ONES2 0x3F803F80u
DEVINL void store_frag16_sc1(__amdgpu_buffer_rsrc_t r, int voff, int soff, const float* v) {
    u32x4 o = {pack_bf16(v[0], v[1]), pack_bf16(v[2], v[3]), pack_bf16(v[4], v[5]), pack_bf16(v[6], v[7])};
    __builtin_amdgcn_raw_buffer_store_b128(o, r, voff, soff, 16);
}

static constexpr int S = 2048, D = 512, F = 1376;
static constexpr int NTT = 64;
static constexpr int NCH = 118;
static constexpr unsigned WS_KVF = 4096, WS_HF = 4096 + 16384, WS_OF = 4096 + 32768, WS_ARR = 4096 + 49152;
static constexpr unsigned WS_Q = 1u << 20, WS_K = 3u << 20, WS_V = 5u << 20, WS_ACT = 7u << 20;

static constexpr int SLOT = 16384;
static constexpr int SCR_CSP = 8 * SLOT;
static constexpr int SCR_AB = 8 * SLOT + 2048;
static constexpr int SCR_WV = 8 * SLOT + 3072;
static constexpr int SCR_TAG = 8 * SLOT + 3072 + 2048;
static constexpr int LDS_BYTES = 8 * SLOT + 3072 + 2048 + 16;

struct Ctx {
    rsrc_t rx, rq, rf1, rf3;
    rsrc_t rK, rV, rA, rw2, rwo;
    __amdgpu_buffer_rsrc_t sQ, sK, sV, sA;
    const char* x; char* out; char* ws;
    unsigned tag;
    int rb, hp, pair0;
    int w, rg, lane;
    int voff_x, voff_qkv, voff_ffn;
};
#define TSTAMP(i) do {} while (0)

DEVINL unsigned* flag_ptr(const Ctx& c, unsigned base, int idx) { return (unsigned*)(c.ws + base + idx * 64); }
DEVINL void flag_set(const Ctx& c, unsigned base, int idx) { __hip_atomic_store(flag_ptr(c, base, idx), c.tag, __ATOMIC_RELAXED, __HIP_MEMORY_SCOPE_AGENT); }
DEVINL void flags_wait(const Ctx& c, unsigned base, int idx0, int stride, int n) {
    unsigned* p = flag_ptr(c, base, idx0 + (c.lane < n ? c.lane : 0) * stride);
    for (int it = 0; it < (1 << 22); ++it) {
        unsigned v = __hip_atomic_load(p, __ATOMIC_RELAXED, __HIP_MEMORY_SCOPE_AGENT);
        if (__all(v == c.tag)) break;
        __builtin_amdgcn_s_sleep(1);
    }
    CBAR();
}

template <int CH, int NP> struct G1 {
    static constexpr int NR = (NP + 1) / 2;
    static constexpr int NITEMS = 16 + 8 * NR;
    static constexpr int issuer(int i) { return i < 24 ? 0 : 1; }
    static constexpr int cnt(int i) {
        if (i >= NITEMS || issuer(i) != CH) return 0;
        if (i < 16) return (i & 1) ? 3 : 4;
        return (2 * ((i - 16) >> 3) + 1 < NP) ? 4 : 2;
    }
    static constexpr int younger(int first, int last) { int n = 0; for (int i = first; i <= last; ++i) n += cnt(i); return n; }
};

DEVINL void g1_issue_x(const Ctx& c, LAS char* sm, int s, int slot) {
#pragma unroll
    for (int m = 0; m < 4; ++m) {
        int id = c.rg + 4 * m;
        blds16<false>(c.rx, sm + slot * SLOT + id * 1024, c.voff_x, (c.rb * 128 + 8 * id) * 1024 + s * 128);
    }
}
DEVINL void g1_issue_wqkv(const Ctx& c, LAS char* sm, int s, int slot) {
#pragma unroll
    for (int m = 0; m < 3; ++m)
        blds16<false>(c.rq, sm + slot * SLOT + (4 * m + c.rg) * 1024, c.voff_qkv, (64 * s + 16 * c.rg) * 3072 + (m * 512 + c.hp * 32) * 2);
}
template <int NP> DEVINL void g1_issue_ffn(const Ctx& c, LAS char* sm, int round, int j) {
#pragma unroll
    for (int m = 0; m < 4; ++m) {
        if (2 * round + (m >> 1) < NP)
            blds16<false>((m & 1) ? c.rf3 : c.rf1, sm + j * SLOT + (4 * m + c.rg) * 1024, c.voff_ffn, (64 * j + 16 * c.rg) * 2752 + (c.pair0 + 2 * round + (m >> 1)) * 64);
    }
}
template <int CH, int NP, int IT> DEVINL void g1_issue_item(const Ctx& c, LAS char* sm) {
    if constexpr (G1<CH, NP>::issuer(IT) != CH) {
    } else if constexpr (IT < 16) {
        if constexpr ((IT & 1) == 0) g1_issue_x(c, sm, IT >> 1, IT & 7);
        else g1_issue_wqkv(c, sm, IT >> 1, IT & 7);
    } else if constexpr (IT < G1<CH, NP>::NITEMS) {
        g1_issue_ffn<NP>(c, sm, (IT - 16) >> 3, (IT - 16) & 7);
    }
}
DEVINL void g1_load_cs(LAS char* sm, int wvoff, int csp_tile, int lane, f32x4 (&c4)[4]) {
    LAS float* csp = (LAS float*)(sm + SCR_CSP) + csp_tile * 128;
    LAS float* csf = (LAS float*)(sm + wvoff);
    int n = lane & 31;
    float v = (csp[n] + csp[32 + n]) + (csp[64 + n] + csp[96 + n]);
    if (lane < 32) csf[n] = v;
    CBAR();
    int h = lane >> 5;
#pragma unroll
    for (int g = 0; g < 4; ++g) c4[g] = *(LAS f32x4*)(csf + 8 * g + 4 * h);
}

template <int CH, int NP>
DEVINL void phase_ab(Ctx& c, LAS char* sm) {
    using G = G1<CH, NP>;
    const int lane = c.lane, rg = c.rg, h = lane >> 5;
    const int wl = (8 * h + ((lane & 15) >> 2)) * 64 + ((lane >> 4) & 1) * 32 + (lane & 3) * 8;
    const int xr = 32 * rg + (lane & 31);
    const int xm = (xr >> 1) & 7;
    const int xa0 = xr * 128 + ((h ^ (xm & 1)) << 4) + ((xm >> 1) << 5);
    const int wvoff = SCR_WV + c.w * 256;

    s16x8 xf[32];
    float s1 = 0.f, s2 = 0.f;
    f32x16 accA = {0}, accB = {0};
    float csA = 0.f, csB = 0.f;
    s16x8 a0, a1, b0, b1;

    static_for<0, 8>([&](auto SI) {
        constexpr int s = SI;
        constexpr int need = s < 7 ? 2 * s + 3 : 16;
        constexpr int issued = s == 0 ? 7 : 2 * s + 5;
#define F1(k) do {} while (0)
        F1(0);
        wait_vm<G::younger(need + 1, issued)>();
        F1(1);
        if constexpr (s == 0) wg_barrier(); else raw_barrier();
        F1(2);
        if constexpr (s == 0) c.tag = *(volatile LAS unsigned*)(sm + SCR_TAG);
        if constexpr (s == 0) TSTAMP(1);
        if constexpr (s >= 1 && CH == 0) { g1_issue_item<CH, NP, 2 * s + 6>(c, sm); g1_issue_item<CH, NP, 2 * s + 7>(c, sm); }
        const LAS char* xs = sm + ((2 * s) & 7) * SLOT;
        const LAS char* wa = sm + ((2 * s + 1) & 7) * SLOT + (CH == 0 ? 2 * 4096 : 0) + wl;
        const LAS char* wb = wa + 4096;
        const LAS char* nxs = sm + ((2 * s + 2) & 7) * SLOT;
        const LAS char* nwa = sm + ((2 * s + 3) & 7) * SLOT + (CH == 0 ? 2 * 4096 : 0) + wl;
        const LAS char* nwb = nwa + 4096;
        if constexpr (s == 0) {
            xf[0] = *(const LAS s16x8*)(xs + xa0);
            xf[1] = *(const LAS s16x8*)(xs + (xa0 ^ 32));
            a0 = wfrag(wa, wa + 256); a1 = wfrag(wa + 1024, wa + 1024 + 256);
            if constexpr (CH == 1) { b0 = wfrag(wb, wb + 256); b1 = wfrag(wb + 1024, wb + 1024 + 256); }
        }
        if constexpr (CH == 1) {
            auto stats = [&](s16x8 v) {
                u32x4 u = __builtin_bit_cast(u32x4, v);
#pragma unroll
                for (int q = 0; q < 4; ++q) { s1 = dot2_acc(u[q], ONES2, s1); s2 = dot2_acc(u[q], u[q], s2); }
            };
            SBAR(); F1(3);
            accA = mfma32(a0, xf[4 * s + 0], accA);
            stats(xf[4 * s + 0]);
            xf[4 * s + 2] = *(const LAS s16x8*)(xs + (xa0 ^ 64));
            s16x8 a2 = wfrag(wa + 2048, wa + 2048 + 256);
            SBAR(); F1(4);
            accB = mfma32(b0, xf[4 * s + 0], accB);
            s16x8 b2 = wfrag(wb + 2048, wb + 2048 + 256);
            s16x8 ca = wfrag(wa + rg * 1024, wa + rg * 1024 + 256);
            SBAR(); F1(5);
            accA = mfma32(a1, xf[4 * s + 1], accA);
            stats(xf[4 * s + 1]);
            xf[4 * s + 3] = *(const LAS s16x8*)(xs + (xa0 ^ 96));
            s16x8 a3 = wfrag(wa + 3072, wa + 3072 + 256);
            SBAR(); F1(6);
            accB = mfma32(b1, xf[4 * s + 1], accB);
            s16x8 b3 = wfrag(wb + 3072, wb + 3072 + 256);
            s16x8 cb = wfrag(wb + rg * 1024, wb + rg * 1024 + 256);
            SBAR(); F1(7);
            accA = mfma32(a2, xf[4 * s + 2], accA);
            stats(xf[4 * s + 2]);
            if constexpr (s < 7) { xf[4 * s + 4] = *(const LAS s16x8*)(nxs + xa0); a0 = wfrag(nwa, nwa + 256); }
            SBAR(); F1(8);
            accB = mfma32(b2, xf[4 * s + 2], accB);
            if constexpr (s < 7) { b0 = wfrag(nwb, nwb + 256); }
            u32x4 cau = __builtin_bit_cast(u32x4, ca);
#pragma unroll
            for (int q = 0; q < 4; ++q) csA = dot2_acc(cau[q], ONES2, csA);
            SBAR(); F1(9);
            accA = mfma32(a3, xf[4 * s + 3], accA);
            stats(xf[4 * s + 3]);
            if constexpr (s < 7) { xf[4 * s + 5] = *(const LAS s16x8*)(nxs + (xa0 ^ 32)); a1 = wfrag(nwa + 1024, nwa + 1024 + 256); }
            SBAR(); F1(10);
            accB = mfma32(b3, xf[4 * s + 3], accB);
            if constexpr (s < 7) { b1 = wfrag(nwb + 1024, nwb + 1024 + 256); }
            u32x4 cbu = __builtin_bit_cast(u32x4, cb);
#pragma unroll
            for (int q = 0; q < 4; ++q) csB = dot2_acc(cbu[q], ONES2, csB);
            SBAR(); F1(11);
            if constexpr (s >= 1) { g1_issue_item<CH, NP, 2 * s + 6>(c, sm); g1_issue_item<CH, NP, 2 * s + 7>(c, sm); }
        } else {
            SBAR(); F1(3);
            accA = mfma32(xf[4 * s + 0], a0, accA);
            xf[4 * s + 2] = *(const LAS s16x8*)(xs + (xa0 ^ 64));
            s16x8 a2 = wfrag(wa + 2048, wa + 2048 + 256);
            s16x8 ca = wfrag(wa + rg * 1024, wa + rg * 1024 + 256);
            SBAR(); F1(4);
            accA = mfma32(xf[4 * s + 1], a1, accA);
            xf[4 * s + 3] = *(const LAS s16x8*)(xs + (xa0 ^ 96));
            s16x8 a3 = wfrag(wa + 3072, wa + 3072 + 256);
            SBAR(); F1(5);
            accA = mfma32(xf[4 * s + 2], a2, accA);
            if constexpr (s < 7) { xf[4 * s + 4] = *(const LAS s16x8*)(nxs + xa0); a0 = wfrag(nwa, nwa + 256); }
            u32x4 cau = __builtin_bit_cast(u32x4, ca);
#pragma unroll
            for (int q = 0; q < 4; ++q) csA = dot2_acc(cau[q], ONES2, csA);
            SBAR(); F1(6);
            accA = mfma32(xf[4 * s + 3], a3, accA);
            if constexpr (s < 7) { xf[4 * s + 5] = *(const LAS s16x8*)(nxs + (xa0 ^ 32)); a1 = wfrag(nwa + 1024, nwa + 1024 + 256); }
            SBAR(); F1(7);
        }
    });
    TSTAMP(2);

    LAS float* csp = (LAS float*)(sm + SCR_CSP);
    LAS float* AB = (LAS float*)(sm + SCR_AB) + rg * 64;
    {
        float pa = csA + __shfl_xor(csA, 32);
        if constexpr (CH == 1) {
            s1 += __shfl_xor(s1, 32);
            s2 += __shfl_xor(s2, 32);
            float mean = s1 * (1.f / 512.f);
            float var = fmaxf(s2 * (1.f / 512.f) - mean * mean, 0.f);
            float rstd = 1.0f / sqrtf(var + 1e-5f);
            float pb = csB + __shfl_xor(csB, 32);
            if (lane < 32) { AB[lane] = rstd; AB[32 + lane] = -mean * rstd; csp[0 * 128 + rg * 32 + lane] = pa; csp[1 * 128 + rg * 32 + lane] = pb; }
        } else {
            if (lane < 32) csp[2 * 128 + rg * 32 + lane] = pa;
        }
    }
    const int tt = 4 * c.rb + rg;
    wait_vm<G::younger(18, 21)>();
    wg_barrier();
    {
        if constexpr (CH == 0) {
            LAS float* cspv = csp + 2 * 128;
            int n = lane & 31;
            float csV = (cspv[n] + cspv[32 + n]) + (cspv[64 + n] + cspv[96 + n]);
            float v[16];
#pragma unroll
            for (int g = 0; g < 4; ++g) {
                f32x4 A4 = *(LAS f32x4*)(AB + 8 * g + 4 * h);
                f32x4 B4 = *(LAS f32x4*)(AB + 32 + 8 * g + 4 * h);
#pragma unroll
                for (int i = 0; i < 4; ++i) v[4 * g + i] = fmaf(accA[4 * g + i], A4[i], csV * B4[i]);
            }
            int soff = c.hp * 2 * NTT * 1024 + tt * 1024;
            int voff = (n >> 4) * NTT * 1024 + (h * 16 + (n & 15)) * 16;
            store_frag16_sc1(c.sV, voff, soff, v);
            store_frag16_sc1(c.sV, voff, soff + 512, v + 8);
        } else {
            const float ra = AB[lane & 31], rbm = AB[32 + (lane & 31)];
            static_for<0, 2>([&](auto TI) {
                constexpr int t = TI;
                f32x4 c4[4];
                g1_load_cs(sm, wvoff + 128 * t, t, lane, c4);
                const f32x16& acc = t == 0 ? accA : accB;
                float v[16];
#pragma unroll
                for (int g = 0; g < 4; ++g)
#pragma unroll
                    for (int i = 0; i < 4; ++i) v[4 * g + i] = fmaf(acc[4 * g + i], ra, rbm * c4[g][i]) * (t == 0 ? 0.36067376022224085f : 1.0f);
                int soff = (2 * c.hp * NTT + tt) * 1024;
                store_frag16_sc1(t == 0 ? c.sQ : c.sK, lane * 16, soff, v);
                store_frag16_sc1(t == 0 ? c.sQ : c.sK, lane * 16, soff + NTT * 1024, v + 8);
            });
        }
    }
    g1_issue_item<CH, NP, 22>(c, sm);
    g1_issue_item<CH, NP, 23>(c, sm);
    TSTAMP(3);
    const float ra = AB[lane & 31], rbm = AB[32 + (lane & 31)];

    s16x8 fG0, fU0, fG1, fU1;
    if constexpr (CH < NP) {
        const LAS char* p = sm + (CH * 2) * 4096 + wl;
        fG0 = wfrag(p, p + 256); fU0 = wfrag(p + 4096, p + 4096 + 256);
        fG1 = wfrag(p + 1024, p + 1024 + 256); fU1 = wfrag(p + 4096 + 1024, p + 4096 + 1024 + 256);
    }
    static_for<0, G::NR>([&](auto RI) {
        constexpr int r = RI;
        constexpr bool active = (2 * r + CH) < NP;
        f32x16 accG = {0}, accU = {0};
        float csG = 0.f, csU = 0.f;
        static_for<0, 8>([&](auto JI) {
            constexpr int j = JI;
            constexpr int i = 16 + 8 * r + j;
#define FS(k) do {} while (0)
            FS(0);
            if constexpr (i > 16) {
                wait_vm<G::younger(i + 2, i + 6)>();
                FS(1);
                raw_barrier();
                FS(2);
                if constexpr (CH == 0) g1_issue_item<CH, NP, i + 7>(c, sm);
            }
            FS(3);
            if constexpr (i == 21) {
                if (threadIdx.x == 0) { flag_set(c, WS_KVF, c.rb * 16 + c.hp); flag_set(c, WS_ARR, blockIdx.x); }
            }
            if constexpr (active) {
                const LAS char* wg_ = sm + j * SLOT + (CH * 2) * 4096 + wl;
                const LAS char* wu_ = wg_ + 4096;
                const LAS char* ng_ = sm + ((j + 1) & 7) * SLOT + (CH * 2) * 4096 + wl;
                const LAS char* nu_ = ng_ + 4096;
                SBAR();
                accG = mfma32(fG0, xf[4 * j + 0], accG);
                s16x8 g2 = wfrag(wg_ + 2048, wg_ + 2048 + 256);
                s16x8 cgf = wfrag(wg_ + rg * 1024, wg_ + rg * 1024 + 256);
                SBAR(); FS(4);
                accU = mfma32(fU0, xf[4 * j + 0], accU);
                s16x8 u2 = wfrag(wu_ + 2048, wu_ + 2048 + 256);
                s16x8 cuf = wfrag(wu_ + rg * 1024, wu_ + rg * 1024 + 256);
                SBAR(); FS(5);
                accG = mfma32(fG1, xf[4 * j + 1], accG);
                s16x8 g3 = wfrag(wg_ + 3072, wg_ + 3072 + 256);
                SBAR(); FS(6);
                accU = mfma32(fU1, xf[4 * j + 1], accU);
                s16x8 u3 = wfrag(wu_ + 3072, wu_ + 3072 + 256);
                SBAR(); FS(7);
                accG = mfma32(g2, xf[4 * j + 2], accG);
                fG0 = wfrag(ng_, ng_ + 256);
                SBAR(); FS(8);
                accU = mfma32(u2, xf[4 * j + 2], accU);
                fU0 = wfrag(nu_, nu_ + 256);
                u32x4 wgq = __builtin_bit_cast(u32x4, cgf), wuq = __builtin_bit_cast(u32x4, cuf);
                csG = dot2_acc(wgq[0], ONES2, csG); csU = dot2_acc(wuq[0], ONES2, csU);
                csG = dot2_acc(wgq[1], ONES2, csG); csU = dot2_acc(wuq[1], ONES2, csU);
                SBAR(); FS(9);
                accG = mfma32(g3, xf[4 * j + 3], accG);
                fG1 = wfrag(ng_ + 1024, ng_ + 1024 + 256);
                csG = dot2_acc(wgq[2], ONES2, csG); csU = dot2_acc(wuq[2], ONES2, csU);
                SBAR(); FS(10);
                accU = mfma32(u3, xf[4 * j + 3], accU);
                fU1 = wfrag(nu_ + 1024, nu_ + 1024 + 256);
                csG = dot2_acc(wgq[3], ONES2, csG); csU = dot2_acc(wuq[3], ONES2, csU);
                SBAR(); FS(11);
            }
            if constexpr (CH == 1 && i > 16) g1_issue_item<CH, NP, i + 7>(c, sm);
        });
        TSTAMP(4 + 2 * r);
        if constexpr (active) {
            float pa = csG + __shfl_xor(csG, 32);
            float pb = csU + __shfl_xor(csU, 32);
            if (lane < 32) { csp[(CH * 2) * 128 + rg * 32 + lane] = pa; csp[(CH * 2 + 1) * 128 + rg * 32 + lane] = pb; }
        }
        wg_barrier();
        if constexpr (active) {
            f32x4 cg4[4], cu4[4];
            g1_load_cs(sm, wvoff, CH * 2, lane, cg4);
            g1_load_cs(sm, wvoff + 128, CH * 2 + 1, lane, cu4);
            float v[16];
#pragma unroll
            for (int g = 0; g < 4; ++g)
#pragma unroll
                for (int i = 0; i < 4; ++i) {
                    float gv = fmaf(accG[4 * g + i], ra, rbm * cg4[g][i]);
                    float uv = fmaf(accU[4 * g + i], ra, rbm * cu4[g][i]);
                    float e = __builtin_amdgcn_exp2f(gv * -1.4426950408889634f);
                    float sg = gv * __builtin_amdgcn_rcpf(1.f + e);
                    v[4 * g + i] = sg * uv;
                }
            constexpr int prel = 2 * r + CH;
            int soff = (tt * NCH + 2 * (c.pair0 + prel)) * 1024;
            store_frag16_sc1(c.sA, lane * 16, soff, v);
            store_frag16_sc1(c.sA, lane * 16, soff + 1024, v + 8);
        }
        TSTAMP(5 + 2 * r);
    });
}

static constexpr int AT_K = 0, AT_V = 24576, AT_ONES = 49152;
DEVINL void phase_attn(const Ctx& c, LAS char* sm) {
    const int hp = c.hp, qb = c.rb, w = c.w, lane = c.lane;
    const int hl = w >> 2, qi = w & 3, head = 2 * hp + hl, qt = 4 * qb + qi;
    const int h = lane >> 5;
#pragma unroll
    for (int m = 0; m < 6; ++m) {
        int id = w + 8 * m;
        int arr = id / 24, hh = (id % 24) / 12, t = id % 12;
        int tt = 4 * qb - 8 + t; if (tt < 0) tt = 0;
        blds16<true>(arr ? c.rV : c.rK, sm + arr * 24576 + (hh * 12 + t) * 1024, lane * 16, ((2 * hp + hh) * NTT + tt) * 1024);
    }
    s16x8 qf;
    {
        const char* qp = c.ws + WS_Q + (size_t)(head * NTT + qt) * 1024 + lane * 16;
        asm volatile("global_load_dwordx4 %0, %1, off sc1" : "=v"(qf) : "v"(qp) : "memory");
    }
    {
        u32x4 ones = {ONES2, ONES2, ONES2, ONES2};
        *(LAS u32x4*)(sm + AT_ONES + threadIdx.x * 16) = ones;
        if (threadIdx.x < 256) *(LAS u32x4*)(sm + AT_ONES + 8192 + threadIdx.x * 16) = ones;
    }
#define FA(k) do {} while (0)
    FA(0);
    asm volatile("s_waitcnt vmcnt(0) lgkmcnt(0)\n\ts_barrier" : "+v"(qf) :: "memory");
    SBAR();
    FA(1);

    const LAS char* kb = sm + AT_K + (hl * 12 + qi) * 1024 + lane * 16;
    const int ql = lane & 31;
    const float NEG = -__builtin_inff();
    f32x16 z0, z8;
#pragma unroll
    for (int r = 0; r < 16; ++r) {
        int kl = (r & 3) + 8 * (r >> 2) + 4 * h;
        z0[r] = (kl > ql) ? 0.f : NEG;
        z8[r] = (kl <= ql) ? 0.f : NEG;
    }
    const int tlo = qt < 8 ? 8 - qt : 0;
    f32x16 ninf;
#pragma unroll
    for (int r = 0; r < 16; ++r) ninf[r] = NEG;
    float m4[4] = {NEG, NEG, NEG, NEG};
    static_for<0, 3>([&](auto GI) {
        constexpr int t0 = 3 * GI;
        f32x16 z = {0};
        f32x16 s[3];
        static_for<0, 3>([&](auto UI) {
            constexpr int t = t0 + UI;
            s16x8 kf = *(const LAS s16x8*)(kb + t * 1024);
            s[UI] = mfma32(kf, qf, t == 0 ? z0 : (t == 8 ? z8 : z));
        });
        static_for<0, 3>([&](auto UI) {
            constexpr int t = t0 + UI;
            float ma = __builtin_fmaxf(__builtin_fmaxf(s[UI][0], s[UI][1]), s[UI][2]);
            float mb = __builtin_fmaxf(__builtin_fmaxf(s[UI][8], s[UI][9]), s[UI][10]);
            ma = __builtin_fmaxf(__builtin_fmaxf(ma, s[UI][3]), s[UI][4]); mb = __builtin_fmaxf(__builtin_fmaxf(mb, s[UI][11]), s[UI][12]);
            ma = __builtin_fmaxf(__builtin_fmaxf(ma, s[UI][5]), s[UI][6]); mb = __builtin_fmaxf(__builtin_fmaxf(mb, s[UI][13]), s[UI][14]);
            ma = __builtin_fmaxf(__builtin_fmaxf(ma, s[UI][7]), mb); ma = __builtin_fmaxf(ma, s[UI][15]);
            if constexpr (t < 8) ma = (t >= tlo) ? ma : NEG;
            m4[UI] = __builtin_fmaxf(m4[UI], ma);
        });
    });
    float m = __builtin_fmaxf(__builtin_fmaxf(m4[0], m4[1]), m4[2]);
    m = fmaxf(m, __shfl_xor(m, 32));
    SBAR(); FA(2); FA(3);
    f32x16 cm;
#pragma unroll
    for (int r = 0; r < 16; ++r) { cm[r] = -m; z0[r] -= m; z8[r] -= m; }
    if (tlo > 0) { z0 = ninf; }
    f32x16 o = {0};
    const LAS char* vb = sm + ((lane & 16) ? AT_ONES : (AT_V + hl * 12288)) + qi * 1024 + (h * 16 + (lane & 15)) * 16;
    f32x16 scur = mfma32(*(const LAS s16x8*)(kb), qf, z0);
    static_for<0, 9>([&](auto TI) {
        constexpr int t = TI;
        f32x16 snxt;
        if constexpr (t < 8) {
            s16x8 kf = *(const LAS s16x8*)(kb + (t + 1) * 1024);
            if constexpr (t + 1 == 8) snxt = mfma32(kf, qf, z8);
            else if (tlo > t + 1) snxt = mfma32(kf, qf, ninf);
            else snxt = mfma32(kf, qf, cm);
        }
        s16x8 vf0 = *(const LAS s16x8*)(vb + t * 1024), vf1 = *(const LAS s16x8*)(vb + t * 1024 + 512);
        float p[16];
#pragma unroll
        for (int r = 0; r < 16; ++r) p[r] = __builtin_amdgcn_exp2f(scur[r]);
        u32x4 pk0 = {pack_bf16(p[0], p[1]), pack_bf16(p[2], p[3]), pack_bf16(p[4], p[5]), pack_bf16(p[6], p[7])};
        o = mfma32(vf0, __builtin_bit_cast(s16x8, pk0), o);
        u32x4 pk1 = {pack_bf16(p[8], p[9]), pack_bf16(p[10], p[11]), pack_bf16(p[12], p[13]), pack_bf16(p[14], p[15])};
        o = mfma32(vf1, __builtin_bit_cast(s16x8, pk1), o);
        if constexpr (t < 8) scur = snxt;
    });
    SBAR(); FA(4);
    float inv = 1.f / o[8];
    float v[8];
#pragma unroll
    for (int r = 0; r < 8; ++r) v[r] = o[r] * inv;
    store_frag16_sc1(c.sA, lane * 16, (qt * NCH + 86 + head) * 1024, v);
}

DEVINL void g3_issue(const Ctx& c, LAS char* sm, int r64, int c64, int voff_w, int st) {
    const int q = c.rg, slot = st & 7;
    int chunk0 = st < 22 ? 4 * st : 86 + 4 * (st - 22);
#pragma unroll
    for (int m = 0; m < 2; ++m) blds16<true>(c.rA, sm + slot * SLOT + (4 * m + q) * 1024, c.lane * 16, ((2 * r64 + m) * NCH + chunk0 + q) * 1024);
#pragma unroll
    for (int m = 0; m < 2; ++m) {
        int k0 = (st < 22 ? 64 * st : 64 * (st - 22)) + 8 * (4 * m + q);
        if (st == 21 && m == 1) k0 = 64 * 21;
        blds16<false>(st < 22 ? c.rw2 : c.rwo, sm + slot * SLOT + 8192 + (4 * m + q) * 1024, voff_w, k0 * 1024 + c64 * 128);
    }
}
DEVINL void phase_out_loader(const Ctx& c, LAS char* sm, int r64, int c64) {
    const int lane = c.lane;
    const int voff_w = (lane >> 3) * 1024 + (((lane & 7) ^ ((((lane >> 3) >> 1) & 1) << 2)) << 4);
    static_for<0, 7>([&](auto SI) { g3_issue(c, sm, r64, c64, voff_w, SI); });
    static_for<0, 30>([&](auto SI) {
        constexpr int st = SI;
        constexpr int last_issued = st + 6 < 29 ? st + 6 : 29;
        constexpr int need = st + 1 < 29 ? st + 1 : 29;
        constexpr int NW = st == 16 ? 4 * (21 - need) : 4 * (last_issued - need);
#define FS3(k) do {} while (0)
        FS3(0);
        wait_vm<NW>();
        FS3(1);
        raw_barrier();
        FS3(2);
        if constexpr (st + 7 < 30) {
            if constexpr (st + 7 == 22) {
                if (c.w == 4) flags_wait(c, WS_OF, (r64 >> 1) * 16, 1, 16);
                if (c.w == 4) g3_issue(c, sm, r64, c64, voff_w, 22);
            } else if constexpr (st + 7 == 23) {
                if (c.w != 4) g3_issue(c, sm, r64, c64, voff_w, 22);
                g3_issue(c, sm, r64, c64, voff_w, 23);
            } else {
                g3_issue(c, sm, r64, c64, voff_w, st + 7);
            }
        }
        FS3(3);
    });
    raw_barrier();
}
DEVINL void phase_out_consumer(const Ctx& c, LAS char* sm, int r64, int c64) {
    const int w = c.w, lane = c.lane;
    const int ttl = w & 1, ct = (w >> 1) & 1, h = lane >> 5;
    const int q = (lane & 15) >> 2, p = lane & 3, grp = (lane >> 4) & 1;
    const int wl = 8192 + (4 * h + q) * 128 + (((ct * 4 + grp * 2 + (p >> 1)) ^ (((q >> 1) & 1) << 2)) << 4) + (p & 1) * 8;
    const int al = ttl * 4096 + lane * 16;
    f32x16 accF = {0}, accS = {0};
    s16x8 fa[4], fw[4];
    static_for<0, 30>([&](auto SI) {
        constexpr int st = SI;
        FS3(0);
        raw_barrier();
        FS3(1);
        const LAS char* sl = sm + (st & 7) * SLOT;
        const LAS char* nsl = sm + ((st + 1) & 7) * SLOT;
        if constexpr (st == 0) {
            static_for<0, 4>([&](auto KI) { constexpr int ks = KI; fa[ks] = *(const LAS s16x8*)(sl + al + ks * 1024); fw[ks] = wfrag(sl + wl + ks * 2048, sl + wl + ks * 2048 + 1024); });
        }
        static_for<0, 4>([&](auto KI) {
            constexpr int ks = KI;
            SBAR();
            if constexpr (!(st == 21 && ks >= 2)) { if constexpr (st < 22) accF = mfma32(fw[ks], fa[ks], accF); else accS = mfma32(fw[ks], fa[ks], accS); }
            if constexpr (st + 1 < 30) { fa[ks] = *(const LAS s16x8*)(nsl + al + ks * 1024); fw[ks] = wfrag(nsl + wl + ks * 2048, nsl + wl + ks * 2048 + 1024); }
            FS3(2 + ks);
        });
        SBAR();
    });
    raw_barrier();
    const int token = 64 * r64 + 32 * ttl + (lane & 31);
#pragma unroll
    for (int g = 0; g < 4; ++g) {
        int col = 64 * c64 + 32 * ct + 8 * g + 4 * h;
        size_t off = ((size_t)token * 512 + col) * 2;
        uint2 xv = *(const uint2*)(c.x + off);
        float xs[4] = {__uint_as_float(xv.x << 16), __uint_as_float(xv.x & 0xffff0000u), __uint_as_float(xv.y << 16), __uint_as_float(xv.y & 0xffff0000u)};
        float r[4];
#pragma unroll
        for (int i = 0; i < 4; ++i) {
            float fv = accF[4 * g + i], sv = accS[4 * g + i];
            float e = __builtin_amdgcn_exp2f(fv * -1.4426950408889634f);
            float gg = fv * __builtin_amdgcn_rcpf(1.f + e);
            r[i] = xs[i] + sv * gg;
        }
        uint2 ov = {pack_bf16(r[0], r[1]), pack_bf16(r[2], r[3])};
        *(uint2*)(c.out + off) = ov;
    }
}

#define PHASE_MASK 7
template <int PMASK>
__global__ __launch_bounds__(512, 2) void fused_kernel(const char* __restrict__ x, const char* __restrict__ wqkv, const char* __restrict__ wo,
                                                       const char* __restrict__ w1, const char* __restrict__ w3, const char* __restrict__ w2,
                                                       char* out, char* ws) {
    extern __shared__ __attribute__((aligned(16))) char smem[];
    LAS char* sm = (LAS char*)smem;
    Ctx c;
    const int b = blockIdx.x, xcd = b & 7, j = b >> 3;
    c.rb = (xcd >> 2) * 8 + (j & 7);
    const int cg = (xcd & 3) * 4 + (j >> 3);
    c.hp = cg;
    const int np = cg < 11 ? 3 : 2;
    c.pair0 = cg < 11 ? 3 * cg : 33 + 2 * (cg - 11);
    c.w = __builtin_amdgcn_readfirstlane(threadIdx.x >> 6);
    c.rg = c.w & 3;
    c.lane = threadIdx.x & 63;
    c.voff_qkv = (c.lane >> 2) * 3072 + (c.lane & 3) * 16;
    c.voff_ffn = (c.lane >> 2) * 2752 + (c.lane & 3) * 16;
    c.voff_x = (c.lane >> 3) * 1024 + (((c.lane & 7) ^ ((4 * (c.w & 1) + (c.lane >> 4)) & 7)) << 4);
    c.rx = mk_rsrc(x, S * D * 2);
    c.rq = mk_rsrc(wqkv, D * 3 * D * 2);
    c.rf1 = mk_rsrc(w1, D * F * 2); c.rf3 = mk_rsrc(w3, D * F * 2);
    c.rK = mk_rsrc(ws + WS_K, 2u << 20); c.rV = mk_rsrc(ws + WS_V, 2u << 20); c.rA = mk_rsrc(ws + WS_ACT, NTT * NCH * 1024);
    c.rw2 = mk_rsrc(w2, F * D * 2); c.rwo = mk_rsrc(wo, D * D * 2);
    c.sQ = __builtin_amdgcn_make_buffer_rsrc(ws + WS_Q, 0, 2u << 20, 0x00020000);
    c.sK = __builtin_amdgcn_make_buffer_rsrc(ws + WS_K, 0, 2u << 20, 0x00020000);
    c.sV = __builtin_amdgcn_make_buffer_rsrc(ws + WS_V, 0, 2u << 20, 0x00020000);
    c.sA = __builtin_amdgcn_make_buffer_rsrc(ws + WS_ACT, 0, NTT * NCH * 1024, 0x00020000);
    c.x = x; c.out = out; c.ws = ws;
    TSTAMP(0);
    if (c.w == 4) {
        unsigned e = __hip_atomic_load((unsigned*)ws, __ATOMIC_RELAXED, __HIP_MEMORY_SCOPE_AGENT);
        if (c.lane == 0) *(volatile LAS unsigned*)(sm + SCR_TAG) = e + 1u;
    }
    if constexpr (!(PMASK & 1)) { wg_barrier(); c.tag = *(volatile LAS unsigned*)(sm + SCR_TAG); }
    if constexpr (PMASK & 1) {
        if (c.w < 4) { static_for<0, 8>([&](auto I) { if (np == 3) g1_issue_item<0, 3, I>(c, sm); else g1_issue_item<0, 2, I>(c, sm); }); }
        else { static_for<0, 8>([&](auto I) { if (np == 3) g1_issue_item<1, 3, I>(c, sm); else g1_issue_item<1, 2, I>(c, sm); }); }
    }
    if constexpr (PMASK & 1) {
        if (c.w < 4) { if (np == 3) phase_ab<0, 3>(c, sm); else phase_ab<0, 2>(c, sm); }
        else { if (np == 3) phase_ab<1, 3>(c, sm); else phase_ab<1, 2>(c, sm); }
        wait_vm<0>();
        raw_barrier();
        if (threadIdx.x == 0) flag_set(c, WS_HF, c.rb * 16 + c.hp);
    }
    TSTAMP(8);
    if constexpr (PMASK & 2) {
        if (c.w == 0) {
            int r0 = c.rb >= 2 ? c.rb - 2 : 0;
            flags_wait(c, WS_KVF, r0 * 16 + c.hp, 16, c.rb - r0 + 1);
        }
        raw_barrier();
        TSTAMP(9);
        phase_attn(c, sm);
        wait_vm<0>();
        raw_barrier();
        if (threadIdx.x == 0) flag_set(c, WS_OF, c.rb * 16 + c.hp);
    }
    TSTAMP(10);
    if constexpr (PMASK & 4) {
        const int r64 = 4 * xcd + (j >> 3), c64 = j & 7;
        if (c.w == 0) flags_wait(c, WS_HF, (r64 >> 1) * 16, 1, 16);
        raw_barrier();
        TSTAMP(11);
        if (c.w < 4) phase_out_consumer(c, sm, r64, c64); else phase_out_loader(c, sm, r64, c64);
        TSTAMP(12);
        if (b == 0 && c.w == 0) {
            for (int g = 0; g < 4; ++g) flags_wait(c, WS_ARR, g * 64, 1, 64);
            if (c.lane == 0) __hip_atomic_store((unsigned*)ws, c.tag, __ATOMIC_RELAXED, __HIP_MEMORY_SCOPE_AGENT);
        }
    }
}

extern "C" void kernel_launch(void* const* d_in, const int* in_sizes, int n_in, void* d_out, int out_size, void* d_ws, size_t ws_size,
                              hipStream_t stream) {
    const char* x = (const char*)d_in[0];
    const char* wqkv = (const char*)d_in[1];
    const char* wo = (const char*)d_in[2];
    const char* w1 = (const char*)d_in[4];
    const char* w3 = (const char*)d_in[5];
    const char* w2 = (const char*)d_in[6];
    hipFuncSetAttribute((const void*)fused_kernel<7>, hipFuncAttributeMaxDynamicSharedMemorySize, LDS_BYTES);
    fused_kernel<7><<<256, 512, LDS_BYTES, stream>>>(x, wqkv, wo, w1, w3, w2, (char*)d_out, (char*)d_ws);
}
```

```cpp
#include <hip/hip_runtime.h>
#include <hip/hip_bf16.h>
#include <stdint.h>

typedef __attribute__((ext_vector_type(8))) short s16x8;
typedef __attribute__((ext_vector_type(4))) short s16x4;
typedef __attribute__((ext_vector_type(16))) float f32x16;
typedef __attribute__((ext_vector_type(4))) float f32x4;
typedef __attribute__((ext_vector_type(4))) unsigned u32x4;
typedef __attribute__((ext_vector_type(2))) __bf16 bf16x2_t;
typedef __attribute__((ext_vector_type(8))) __bf16 bf16x8_t;
typedef __attribute__((ext_vector_type(2))) float f32x2;
typedef __attribute__((ext_vector_type(4))) int rsrc_t;
#define LAS __attribute__((address_space(3)))
#define DEVINL __device__ __forceinline__
#define SBAR() __builtin_amdgcn_sched_barrier(0)
#define CBAR() asm volatile("" ::: "memory")

template <int I> struct IC { static constexpr int value = I; constexpr operator int() const { return I; } };
template <int B, int E, class F> DEVINL void static_for(F&& f) { if constexpr (B < E) { f(IC<B>{}); static_for<B + 1, E>(f); } }

template <int N> DEVINL void wait_vm() { asm volatile("s_waitcnt vmcnt(%0)" ::"n"(N) : "memory"); }
DEVINL void raw_barrier() { asm volatile("s_barrier" ::: "memory"); }
DEVINL void wg_barrier() { asm volatile("s_waitcnt lgkmcnt(0)\n\ts_barrier" ::: "memory"); }

DEVINL rsrc_t mk_rsrc(const void* p, unsigned nbytes) {
    unsigned long long a = (unsigned long long)p;
    rsrc_t r = {(int)(unsigned)a, (int)(unsigned)(a >> 32), (int)nbytes, 0x00020000};
    return r;
}
template <bool SC1> DEVINL void blds16(rsrc_t r, LAS void* ldst, int voff, int soff) {
    if constexpr (SC1)
        asm volatile("s_mov_b32 m0, %0\n\ts_nop 0\n\tbuffer_load_dwordx4 %1, %2, %3 offen sc1 lds"
                     : : "s"(__builtin_amdgcn_readfirstlane((unsigned)(size_t)ldst)), "v"(voff), "s"(r), "s"(__builtin_amdgcn_readfirstlane(soff)) : "memory");
    else
        asm volatile("s_mov_b32 m0, %0\n\ts_nop 0\n\tbuffer_load_dwordx4 %1, %2, %3 offen lds"
                     : : "s"(__builtin_amdgcn_readfirstlane((unsigned)(size_t)ldst)), "v"(voff), "s"(r), "s"(__builtin_amdgcn_readfirstlane(soff)) : "memory");
}
DEVINL f32x16 mfma32(s16x8 a, s16x8 b, f32x16 c) {
    return __builtin_amdgcn_mfma_f32_32x32x16_bf16(__builtin_bit_cast(bf16x8_t, a), __builtin_bit_cast(bf16x8_t, b), c, 0, 0, 0);
}
DEVINL s16x4 tr_read(const LAS char* p) { return __builtin_amdgcn_ds_read_tr16_b64_v4i16((LAS s16x4*)p); }
DEVINL s16x8 wfrag(const LAS char* p0, const LAS char* p1) {
    s16x4 a = tr_read(p0), b = tr_read(p1);
    return __builtin_shufflevector(a, b, 0, 1, 2, 3, 4, 5, 6, 7);
}
DEVINL unsigned pack_bf16(float lo, float hi) {
    f32x2 v = {lo, hi};
    return __builtin_bit_cast(unsigned, __builtin_convertvector(v, bf16x2_t));
}
DEVINL float dot2_acc(unsigned a, unsigned b, float c) {
    return __builtin_amdgcn_fdot2_f32_bf16(__builtin_bit_cast(bf16x2_t, a), __builtin_bit_cast(bf16x2_t, b), c, false);
}
#define ONES2 0x3F803F80u
DEVINL void store_frag16_sc1(__amdgpu_buffer_rsrc_t r, int voff, int soff, const float* v) {
    u32x4 o = {pack_bf16(v[0], v[1]), pack_bf16(v[2], v[3]), pack_bf16(v[4], v[5]), pack_bf16(v[6], v[7])};
    __builtin_amdgcn_raw_buffer_store_b128(o, r, voff, soff, 16);
}

static constexpr int S = 2048, D = 512, F = 1376;
static constexpr int NTT = 64;
static constexpr int NCH = 118;
static constexpr unsigned WS_KVF = 4096, WS_HF = 4096 + 16384, WS_OF = 4096 + 32768, WS_ARR = 4096 + 49152;
static constexpr unsigned WS_Q = 1u << 20, WS_K = 3u << 20, WS_V = 5u << 20, WS_ACT = 7u << 20;

static constexpr int SLOT = 16384;
static constexpr int SCR_CSP = 8 * SLOT;
static constexpr int SCR_AB = 8 * SLOT + 2048;
static constexpr int SCR_WV = 8 * SLOT + 3072;
static constexpr int SCR_TAG = 8 * SLOT + 3072 + 2048;
static constexpr int LDS_BYTES = 8 * SLOT + 3072 + 2048 + 16;

struct Ctx {
    rsrc_t rx, rq, rf1, rf3;
    rsrc_t rK, rV, rA, rw2, rwo;
    __amdgpu_buffer_rsrc_t sQ, sK, sV, sA;
    const char* x; char* out; char* ws;
    unsigned tag;
    int rb, hp, pair0;
    int w, rg, lane;
    int voff_x, voff_qkv, voff_ffn;
};
#define TSTAMP(i) do {} while (0)

DEVINL unsigned* flag_ptr(const Ctx& c, unsigned base, int idx) { return (unsigned*)(c.ws + base + idx * 64); }
DEVINL void flag_set(const Ctx& c, unsigned base, int idx) { __hip_atomic_store(flag_ptr(c, base, idx), c.tag, __ATOMIC_RELAXED, __HIP_MEMORY_SCOPE_AGENT); }
DEVINL void flags_wait(const Ctx& c, unsigned base, int idx0, int stride, int n) {
    unsigned* p = flag_ptr(c, base, idx0 + (c.lane < n ? c.lane : 0) * stride);
    for (int it = 0; it < (1 << 15); ++it) {
        unsigned v = __hip_atomic_load(p, __ATOMIC_RELAXED, __HIP_MEMORY_SCOPE_AGENT);
        if (__all(v == c.tag)) break;
        __builtin_amdgcn_s_sleep(1);
    }
    CBAR();
}

template <int CH, int NP> struct G1 {
    static constexpr int NR = (NP + 1) / 2;
    static constexpr int NITEMS = 16 + 8 * NR;
    static constexpr int issuer(int i) { return i < 24 ? 0 : 1; }
    static constexpr int cnt(int i) {
        if (i >= NITEMS || issuer(i) != CH) return 0;
        if (i < 16) return (i & 1) ? 3 : 4;
        return (2 * ((i - 16) >> 3) + 1 < NP) ? 4 : 2;
    }
    static constexpr int younger(int first, int last) { int n = 0; for (int i = first; i <= last; ++i) n += cnt(i); return n; }
};

DEVINL void g1_issue_x(const Ctx& c, LAS char* sm, int s, int slot) {
#pragma unroll
    for (int m = 0; m < 4; ++m) {
        int id = c.rg + 4 * m;
        blds16<false>(c.rx, sm + slot * SLOT + id * 1024, c.voff_x, (c.rb * 128 + 8 * id) * 1024 + s * 128);
    }
}
DEVINL void g1_issue_wqkv(const Ctx& c, LAS char* sm, int s, int slot) {
#pragma unroll
    for (int m = 0; m < 3; ++m)
        blds16<false>(c.rq, sm + slot * SLOT + (4 * m + c.rg) * 1024, c.voff_qkv, (64 * s + 16 * c.rg) * 3072 + (m * 512 + c.hp * 32) * 2);
}
template <int NP> DEVINL void g1_issue_ffn(const Ctx& c, LAS char* sm, int round, int j) {
#pragma unroll
    for (int m = 0; m < 4; ++m) {
        if (2 * round + (m >> 1) < NP)
            blds16<false>((m & 1) ? c.rf3 : c.rf1, sm + j * SLOT + (4 * m + c.rg) * 1024, c.voff_ffn, (64 * j + 16 * c.rg) * 2752 + (c.pair0 + 2 * round + (m >> 1)) * 64);
    }
}
template <int CH, int NP, int IT> DEVINL void g1_issue_item(const Ctx& c, LAS char* sm) {
    if constexpr (G1<CH, NP>::issuer(IT) != CH) {
    } else if constexpr (IT < 16) {
        if constexpr ((IT & 1) == 0) g1_issue_x(c, sm, IT >> 1, IT & 7);
        else g1_issue_wqkv(c, sm, IT >> 1, IT & 7);
    } else if constexpr (IT < G1<CH, NP>::NITEMS) {
        g1_issue_ffn<NP>(c, sm, (IT - 16) >> 3, (IT - 16) & 7);
    }
}
DEVINL void g1_load_cs(LAS char* sm, int wvoff, int csp_tile, int lane, f32x4 (&c4)[4]) {
    LAS float* csp = (LAS float*)(sm + SCR_CSP) + csp_tile * 128;
    LAS float* csf = (LAS float*)(sm + wvoff);
    int n = lane & 31;
    float v = (csp[n] + csp[32 + n]) + (csp[64 + n] + csp[96 + n]);
    if (lane < 32) csf[n] = v;
    CBAR();
    int h = lane >> 5;
#pragma unroll
    for (int g = 0; g < 4; ++g) c4[g] = *(LAS f32x4*)(csf + 8 * g + 4 * h);
}

template <int CH, int NP>
DEVINL void phase_ab(Ctx& c, LAS char* sm) {
    using G = G1<CH, NP>;
    const int lane = c.lane, rg = c.rg, h = lane >> 5;
    const int wl = (8 * h + ((lane & 15) >> 2)) * 64 + ((lane >> 4) & 1) * 32 + (lane & 3) * 8;
    const int xr = 32 * rg + (lane & 31);
    const int xm = (xr >> 1) & 7;
    const int xa0 = xr * 128 + ((h ^ (xm & 1)) << 4) + ((xm >> 1) << 5);
    const int wvoff = SCR_WV + c.w * 256;

    s16x8 xf[32];
    float s1 = 0.f, s2 = 0.f;
    f32x16 accA = {0}, accB = {0};
    float csA = 0.f, csB = 0.f;
    s16x8 a0, a1, b0, b1;

    static_for<0, 8>([&](auto SI) {
        constexpr int s = SI;
        constexpr int need = s < 7 ? 2 * s + 3 : 16;
        constexpr int issued = s == 0 ? 7 : 2 * s + 5;
#define F1(k) do {} while (0)
        F1(0);
        wait_vm<G::younger(need + 1, issued)>();
        F1(1);
        if constexpr (s == 0) wg_barrier(); else raw_barrier();
        F1(2);
        if constexpr (s == 0) c.tag = *(volatile LAS unsigned*)(sm + SCR_TAG);
        if constexpr (s == 0) TSTAMP(1);
        if constexpr (s >= 1 && CH == 0) { g1_issue_item<CH, NP, 2 * s + 6>(c, sm); g1_issue_item<CH, NP, 2 * s + 7>(c, sm); }
        const LAS char* xs = sm + ((2 * s) & 7) * SLOT;
        const LAS char* wa = sm + ((2 * s + 1) & 7) * SLOT + (CH == 0 ? 2 * 4096 : 0) + wl;
        const LAS char* wb = wa + 4096;
        const LAS char* nxs = sm + ((2 * s + 2) & 7) * SLOT;
        const LAS char* nwa = sm + ((2 * s + 3) & 7) * SLOT + (CH == 0 ? 2 * 4096 : 0) + wl;
        const LAS char* nwb = nwa + 4096;
        if constexpr (s == 0) {
            xf[0] = *(const LAS s16x8*)(xs + xa0);
            xf[1] = *(const LAS s16x8*)(xs + (xa0 ^ 32));
            a0 = wfrag(wa, wa + 256); a1 = wfrag(wa + 1024, wa + 1024 + 256);
            if constexpr (CH == 1) { b0 = wfrag(wb, wb + 256); b1 = wfrag(wb + 1024, wb + 1024 + 256); }
        }
        if constexpr (CH == 1) {
            auto stats = [&](s16x8 v) {
                u32x4 u = __builtin_bit_cast(u32x4, v);
#pragma unroll
                for (int q = 0; q < 4; ++q) { s1 = dot2_acc(u[q], ONES2, s1); s2 = dot2_acc(u[q], u[q], s2); }
            };
            SBAR(); F1(3);
            accA = mfma32(a0, xf[4 * s + 0], accA);
            stats(xf[4 * s + 0]);
            xf[4 * s + 2] = *(const LAS s16x8*)(xs + (xa0 ^ 64));
            s16x8 a2 = wfrag(wa + 2048, wa + 2048 + 256);
            SBAR(); F1(4);
            accB = mfma32(b0, xf[4 * s + 0], accB);
            s16x8 b2 = wfrag(wb + 2048, wb + 2048 + 256);
            s16x8 ca = wfrag(wa + rg * 1024, wa + rg * 1024 + 256);
            SBAR(); F1(5);
            accA = mfma32(a1, xf[4 * s + 1], accA);
            stats(xf[4 * s + 1]);
            xf[4 * s + 3] = *(const LAS s16x8*)(xs + (xa0 ^ 96));
            s16x8 a3 = wfrag(wa + 3072, wa + 3072 + 256);
            SBAR(); F1(6);
            accB = mfma32(b1, xf[4 * s + 1], accB);
            s16x8 b3 = wfrag(wb + 3072, wb + 3072 + 256);
            s16x8 cb = wfrag(wb + rg * 1024, wb + rg * 1024 + 256);
            SBAR(); F1(7);
            accA = mfma32(a2, xf[4 * s + 2], accA);
            stats(xf[4 * s + 2]);
            if constexpr (s < 7) { xf[4 * s + 4] = *(const LAS s16x8*)(nxs + xa0); a0 = wfrag(nwa, nwa + 256); }
            SBAR(); F1(8);
            accB = mfma32(b2, xf[4 * s + 2], accB);
            if constexpr (s < 7) { b0 = wfrag(nwb, nwb + 256); }
            u32x4 cau = __builtin_bit_cast(u32x4, ca);
#pragma unroll
            for (int q = 0; q < 4; ++q) csA = dot2_acc(cau[q], ONES2, csA);
            SBAR(); F1(9);
            accA = mfma32(a3, xf[4 * s + 3], accA);
            stats(xf[4 * s + 3]);
            if constexpr (s < 7) { xf[4 * s + 5] = *(const LAS s16x8*)(nxs + (xa0 ^ 32)); a1 = wfrag(nwa + 1024, nwa + 1024 + 256); }
            SBAR(); F1(10);
            accB = mfma32(b3, xf[4 * s + 3], accB);
            if constexpr (s < 7) { b1 = wfrag(nwb + 1024, nwb + 1024 + 256); }
            u32x4 cbu = __builtin_bit_cast(u32x4, cb);
#pragma unroll
            for (int q = 0; q < 4; ++q) csB = dot2_acc(cbu[q], ONES2, csB);
            SBAR(); F1(11);
            if constexpr (s >= 1) { g1_issue_item<CH, NP, 2 * s + 6>(c, sm); g1_issue_item<CH, NP, 2 * s + 7>(c, sm); }
        } else {
            SBAR(); F1(3);
            accA = mfma32(xf[4 * s + 0], a0, accA);
            xf[4 * s + 2] = *(const LAS s16x8*)(xs + (xa0 ^ 64));
            s16x8 a2 = wfrag(wa + 2048, wa + 2048 + 256);
            s16x8 ca = wfrag(wa + rg * 1024, wa + rg * 1024 + 256);
            SBAR(); F1(4);
            accA = mfma32(xf[4 * s + 1], a1, accA);
            xf[4 * s + 3] = *(const LAS s16x8*)(xs + (xa0 ^ 96));
            s16x8 a3 = wfrag(wa + 3072, wa + 3072 + 256);
            SBAR(); F1(5);
            accA = mfma32(xf[4 * s + 2], a2, accA);
            if constexpr (s < 7) { xf[4 * s + 4] = *(const LAS s16x8*)(nxs + xa0); a0 = wfrag(nwa, nwa + 256); }
            u32x4 cau = __builtin_bit_cast(u32x4, ca);
#pragma unroll
            for (int q = 0; q < 4; ++q) csA = dot2_acc(cau[q], ONES2, csA);
            SBAR(); F1(6);
            accA = mfma32(xf[4 * s + 3], a3, accA);
            if constexpr (s < 7) { xf[4 * s + 5] = *(const LAS s16x8*)(nxs + (xa0 ^ 32)); a1 = wfrag(nwa + 1024, nwa + 1024 + 256); }
            SBAR(); F1(7);
        }
    });
    TSTAMP(2);

    LAS float* csp = (LAS float*)(sm + SCR_CSP);
    LAS float* AB = (LAS float*)(sm + SCR_AB) + rg * 64;
    {
        float pa = csA + __shfl_xor(csA, 32);
        if constexpr (CH == 1) {
            s1 += __shfl_xor(s1, 32);
            s2 += __shfl_xor(s2, 32);
            float mean = s1 * (1.f / 512.f);
            float var = fmaxf(s2 * (1.f / 512.f) - mean * mean, 0.f);
            float rstd = 1.0f / sqrtf(var + 1e-5f);
            float pb = csB + __shfl_xor(csB, 32);
            if (lane < 32) { AB[lane] = rstd; AB[32 + lane] = -mean * rstd; csp[0 * 128 + rg * 32 + lane] = pa; csp[1 * 128 + rg * 32 + lane] = pb; }
        } else {
            if (lane < 32) csp[2 * 128 + rg * 32 + lane] = pa;
        }
    }
    const int tt = 4 * c.rb + rg;
    wait_vm<G::younger(18, 21)>();
    wg_barrier();
    {
        if constexpr (CH == 0) {
            LAS float* cspv = csp + 2 * 128;
            int n = lane & 31;
            float csV = (cspv[n] + cspv[32 + n]) + (cspv[64 + n] + cspv[96 + n]);
            float v[16];
#pragma unroll
            for (int g = 0; g < 4; ++g) {
                f32x4 A4 = *(LAS f32x4*)(AB + 8 * g + 4 * h);
                f32x4 B4 = *(LAS f32x4*)(AB + 32 + 8 * g + 4 * h);
#pragma unroll
                for (int i = 0; i < 4; ++i) v[4 * g + i] = fmaf(accA[4 * g + i], A4[i], csV * B4[i]);
            }
            int soff = c.hp * 2 * NTT * 1024 + tt * 1024;
            int voff = (n >> 4) * NTT * 1024 + (h * 16 + (n & 15)) * 16;
            store_frag16_sc1(c.sV, voff, soff, v);
            store_frag16_sc1(c.sV, voff, soff + 512, v + 8);
        } else {
            const float ra = AB[lane & 31], rbm = AB[32 + (lane & 31)];
            static_for<0, 2>([&](auto TI) {
                constexpr int t = TI;
                f32x4 c4[4];
                g1_load_cs(sm, wvoff + 128 * t, t, lane, c4);
                const f32x16& acc = t == 0 ? accA : accB;
                float v[16];
#pragma unroll
                for (int g = 0; g < 4; ++g)
#pragma unroll
                    for (int i = 0; i < 4; ++i) v[4 * g + i] = fmaf(acc[4 * g + i], ra, rbm * c4[g][i]);
                int soff = (2 * c.hp * NTT + tt) * 1024;
                store_frag16_sc1(t == 0 ? c.sQ : c.sK, lane * 16, soff, v);
                store_frag16_sc1(t == 0 ? c.sQ : c.sK, lane * 16, soff + NTT * 1024, v + 8);
            });
        }
    }
    g1_issue_item<CH, NP, 22>(c, sm);
    g1_issue_item<CH, NP, 23>(c, sm);
    TSTAMP(3);
    const float ra = AB[lane & 31], rbm = AB[32 + (lane & 31)];

    s16x8 fG0, fU0, fG1, fU1;
    if constexpr (CH < NP) {
        const LAS char* p = sm + (CH * 2) * 4096 + wl;
        fG0 = wfrag(p, p + 256); fU0 = wfrag(p + 4096, p + 4096 + 256);
        fG1 = wfrag(p + 1024, p + 1024 + 256); fU1 = wfrag(p + 4096 + 1024, p + 4096 + 1024 + 256);
    }
    static_for<0, G::NR>([&](auto RI) {
        constexpr int r = RI;
        constexpr bool active = (2 * r + CH) < NP;
        f32x16 accG = {0}, accU = {0};
        float csG = 0.f, csU = 0.f;
        static_for<0, 8>([&](auto JI) {
            constexpr int j = JI;
            constexpr int i = 16 + 8 * r + j;
#define FS(k) do {} while (0)
            FS(0);
            if constexpr (i > 16) {
                wait_vm<G::younger(i + 2, i + 6)>();
                FS(1);
                raw_barrier();
                FS(2);
                if constexpr (CH == 0) g1_issue_item<CH, NP, i + 7>(c, sm);
            }
            FS(3);
            if constexpr (i == 21) {
                if (threadIdx.x == 0) { flag_set(c, WS_KVF, c.rb * 16 + c.hp); flag_set(c, WS_ARR, blockIdx.x); }
            }
            if constexpr (active) {
                const LAS char* wg_ = sm + j * SLOT + (CH * 2) * 4096 + wl;
                const LAS char* wu_ = wg_ + 4096;
                const LAS char* ng_ = sm + ((j + 1) & 7) * SLOT + (CH * 2) * 4096 + wl;
                const LAS char* nu_ = ng_ + 4096;
                SBAR();
                accG = mfma32(fG0, xf[4 * j + 0], accG);
                s16x8 g2 = wfrag(wg_ + 2048, wg_ + 2048 + 256);
                s16x8 cgf = wfrag(wg_ + rg * 1024, wg_ + rg * 1024 + 256);
                SBAR(); FS(4);
                accU = mfma32(fU0, xf[4 * j + 0], accU);
                s16x8 u2 = wfrag(wu_ + 2048, wu_ + 2048 + 256);
                s16x8 cuf = wfrag(wu_ + rg * 1024, wu_ + rg * 1024 + 256);
                SBAR(); FS(5);
                accG = mfma32(fG1, xf[4 * j + 1], accG);
                s16x8 g3 = wfrag(wg_ + 3072, wg_ + 3072 + 256);
                SBAR(); FS(6);
                accU = mfma32(fU1, xf[4 * j + 1], accU);
                s16x8 u3 = wfrag(wu_ + 3072, wu_ + 3072 + 256);
                SBAR(); FS(7);
                accG = mfma32(g2, xf[4 * j + 2], accG);
                fG0 = wfrag(ng_, ng_ + 256);
                SBAR(); FS(8);
                accU = mfma32(u2, xf[4 * j + 2], accU);
                fU0 = wfrag(nu_, nu_ + 256);
                u32x4 wgq = __builtin_bit_cast(u32x4, cgf), wuq = __builtin_bit_cast(u32x4, cuf);
                csG = dot2_acc(wgq[0], ONES2, csG); csU = dot2_acc(wuq[0], ONES2, csU);
                csG = dot2_acc(wgq[1], ONES2, csG); csU = dot2_acc(wuq[1], ONES2, csU);
                SBAR(); FS(9);
                accG = mfma32(g3, xf[4 * j + 3], accG);
                fG1 = wfrag(ng_ + 1024, ng_ + 1024 + 256);
                csG = dot2_acc(wgq[2], ONES2, csG); csU = dot2_acc(wuq[2], ONES2, csU);
                SBAR(); FS(10);
                accU = mfma32(u3, xf[4 * j + 3], accU);
                fU1 = wfrag(nu_ + 1024, nu_ + 1024 + 256);
                csG = dot2_acc(wgq[3], ONES2, csG); csU = dot2_acc(wuq[3], ONES2, csU);
                SBAR(); FS(11);
            }
            if constexpr (CH == 1 && i > 16) g1_issue_item<CH, NP, i + 7>(c, sm);
        });
        TSTAMP(4 + 2 * r);
        if constexpr (active) {
            float pa = csG + __shfl_xor(csG, 32);
            float pb = csU + __shfl_xor(csU, 32);
            if (lane < 32) { csp[(CH * 2) * 128 + rg * 32 + lane] = pa; csp[(CH * 2 + 1) * 128 + rg * 32 + lane] = pb; }
        }
        wg_barrier();
        if constexpr (active) {
            f32x4 cg4[4], cu4[4];
            g1_load_cs(sm, wvoff, CH * 2, lane, cg4);
            g1_load_cs(sm, wvoff + 128, CH * 2 + 1, lane, cu4);
            float v[16];
#pragma unroll
            for (int g = 0; g < 4; ++g)
#pragma unroll
                for (int i = 0; i < 4; ++i) {
                    float gv = fmaf(accG[4 * g + i], ra, rbm * cg4[g][i]);
                    float uv = fmaf(accU[4 * g + i], ra, rbm * cu4[g][i]);
                    float e = __builtin_amdgcn_exp2f(gv * -1.4426950408889634f);
                    float sg = gv * __builtin_amdgcn_rcpf(1.f + e);
                    v[4 * g + i] = sg * uv;
                }
            constexpr int prel = 2 * r + CH;
            int soff = (tt * NCH + 2 * (c.pair0 + prel)) * 1024;
            store_frag16_sc1(c.sA, lane * 16, soff, v);
            store_frag16_sc1(c.sA, lane * 16, soff + 1024, v + 8);
        }
        TSTAMP(5 + 2 * r);
    });
}

static constexpr int AT_K = 0, AT_V = 24576, AT_ONES = 49152;
DEVINL void phase_attn(const Ctx& c, LAS char* sm) {
    const int hp = c.hp, qb = c.rb, w = c.w, lane = c.lane;
    const int hl = w >> 2, qi = w & 3, head = 2 * hp + hl, qt = 4 * qb + qi;
    const int h = lane >> 5;
#pragma unroll
    for (int m = 0; m < 6; ++m) {
        int id = w + 8 * m;
        int arr = id / 24, hh = (id % 24) / 12, t = id % 12;
        int tt = 4 * qb - 8 + t; if (tt < 0) tt = 0;
        blds16<true>(arr ? c.rV : c.rK, sm + arr * 24576 + (hh * 12 + t) * 1024, lane * 16, ((2 * hp + hh) * NTT + tt) * 1024);
    }
    s16x8 qf;
    {
        const char* qp = c.ws + WS_Q + (size_t)(head * NTT + qt) * 1024 + lane * 16;
        asm volatile("global_load_dwordx4 %0, %1, off sc1" : "=v"(qf) : "v"(qp) : "memory");
    }
    {
        u32x4 ones = {ONES2, ONES2, ONES2, ONES2};
        *(LAS u32x4*)(sm + AT_ONES + threadIdx.x * 16) = ones;
        if (threadIdx.x < 256) *(LAS u32x4*)(sm + AT_ONES + 8192 + threadIdx.x * 16) = ones;
    }
#define FA(k) do {} while (0)
    FA(0);
    asm volatile("s_waitcnt vmcnt(0) lgkmcnt(0)\n\ts_barrier" : "+v"(qf) :: "memory");
    SBAR();
    FA(1);

    f32x16 s[9];
    const LAS char* kb = sm + AT_K + (hl * 12 + qi) * 1024 + lane * 16;
    static_for<0, 9>([&](auto TI) {
        constexpr int t = TI;
        s16x8 kf = *(const LAS s16x8*)(kb + t * 1024);
        f32x16 z = {0};
        s[t] = mfma32(kf, qf, z);
    });
    SBAR(); FA(2);
    const int ql = lane & 31;
    const float NEG = -__builtin_inff();
#pragma unroll
    for (int r = 0; r < 16; ++r) {
        int kl = (r & 3) + 8 * (r >> 2) + 4 * h;
        s[0][r] = (kl > ql) ? s[0][r] : NEG;
        s[8][r] = (kl <= ql) ? s[8][r] : NEG;
    }
    if (qt < 8) {
        static_for<0, 8>([&](auto TI) {
            constexpr int t = TI;
            if (qt - 8 + t < 0) {
#pragma unroll
                for (int r = 0; r < 16; ++r) s[t][r] = NEG;
            }
        });
    }
    float m = s[8][0];
    static_for<0, 9>([&](auto TI) {
        constexpr int t = TI;
#pragma unroll
        for (int r = 0; r < 16; r += 2) m = __builtin_fmaxf(m, __builtin_fmaxf(s[t][r], s[t][r + 1]));
    });
    m = fmaxf(m, __shfl_xor(m, 32));
    const float cs = 0.25f * 1.4426950408889634f;
    const float mc = -m * cs;
    SBAR(); FA(3);
    f32x16 o = {0};
    const LAS char* vb = sm + ((lane & 16) ? AT_ONES : (AT_V + hl * 12288)) + qi * 1024 + (h * 16 + (lane & 15)) * 16;
    static_for<0, 9>([&](auto TI) {
        constexpr int t = TI;
        float p[16];
#pragma unroll
        for (int r = 0; r < 16; ++r) p[r] = __builtin_amdgcn_exp2f(fmaf(s[t][r], cs, mc));
#pragma unroll
        for (int k2 = 0; k2 < 2; ++k2) {
            u32x4 pk = {pack_bf16(p[8 * k2 + 0], p[8 * k2 + 1]), pack_bf16(p[8 * k2 + 2], p[8 * k2 + 3]),
                        pack_bf16(p[8 * k2 + 4], p[8 * k2 + 5]), pack_bf16(p[8 * k2 + 6], p[8 * k2 + 7])};
            s16x8 vf = *(const LAS s16x8*)(vb + t * 1024 + k2 * 512);
            o = mfma32(vf, __builtin_bit_cast(s16x8, pk), o);
        }
    });
    SBAR(); FA(4);
    float inv = 1.f / o[8];
    float v[8];
#pragma unroll
    for (int r = 0; r < 8; ++r) v[r] = o[r] * inv;
    store_frag16_sc1(c.sA, lane * 16, (qt * NCH + 86 + head) * 1024, v);
}

DEVINL void g3_issue(const Ctx& c, LAS char* sm, int r64, int c64, int voff_w, int st) {
    const int q = c.rg, slot = st & 7;
    int chunk0 = st < 22 ? 4 * st : 86 + 4 * (st - 22);
#pragma unroll
    for (int m = 0; m < 2; ++m) blds16<true>(c.rA, sm + slot * SLOT + (4 * m + q) * 1024, c.lane * 16, ((2 * r64 + m) * NCH + chunk0 + q) * 1024);
#pragma unroll
    for (int m = 0; m < 2; ++m) {
        int k0 = (st < 22 ? 64 * st : 64 * (st - 22)) + 8 * (4 * m + q);
        if (st == 21 && m == 1) k0 = 64 * 21;
        blds16<false>(st < 22 ? c.rw2 : c.rwo, sm + slot * SLOT + 8192 + (4 * m + q) * 1024, voff_w, k0 * 1024 + c64 * 128);
    }
}
DEVINL void phase_out_loader(const Ctx& c, LAS char* sm, int r64, int c64) {
    const int lane = c.lane;
    const int voff_w = (lane >> 3) * 1024 + (((lane & 7) ^ ((((lane >> 3) >> 1) & 1) << 2)) << 4);
    static_for<0, 7>([&](auto SI) { g3_issue(c, sm, r64, c64, voff_w, SI); });
    static_for<0, 30>([&](auto SI) {
        constexpr int st = SI;
        constexpr int last_issued = st + 6 < 29 ? st + 6 : 29;
        constexpr int need = st + 1 < 29 ? st + 1 : 29;
        constexpr int NW = st == 16 ? 4 * (21 - need) : 4 * (last_issued - need);
#define FS3(k) do {} while (0)
        FS3(0);
        wait_vm<NW>();
        FS3(1);
        raw_barrier();
        FS3(2);
        if constexpr (st + 7 < 30) {
            if constexpr (st + 7 == 22) {
                if (c.w == 4) flags_wait(c, WS_OF, (r64 >> 1) * 16, 1, 16);
                if (c.w == 4) g3_issue(c, sm, r64, c64, voff_w, 22);
            } else if constexpr (st + 7 == 23) {
                if (c.w != 4) g3_issue(c, sm, r64, c64, voff_w, 22);
                g3_issue(c, sm, r64, c64, voff_w, 23);
            } else {
                g3_issue(c, sm, r64, c64, voff_w, st + 7);
            }
        }
        FS3(3);
    });
    raw_barrier();
}
DEVINL void phase_out_consumer(const Ctx& c, LAS char* sm, int r64, int c64) {
    const int w = c.w, lane = c.lane;
    const int ttl = w & 1, ct = (w >> 1) & 1, h = lane >> 5;
    const int q = (lane & 15) >> 2, p = lane & 3, grp = (lane >> 4) & 1;
    const int wl = 8192 + (4 * h + q) * 128 + (((ct * 4 + grp * 2 + (p >> 1)) ^ (((q >> 1) & 1) << 2)) << 4) + (p & 1) * 8;
    const int al = ttl * 4096 + lane * 16;
    f32x16 accF = {0}, accS = {0};
    s16x8 fa[4], fw[4];
    static_for<0, 30>([&](auto SI) {
        constexpr int st = SI;
        FS3(0);
        raw_barrier();
        FS3(1);
        const LAS char* sl = sm + (st & 7) * SLOT;
        const LAS char* nsl = sm + ((st + 1) & 7) * SLOT;
        if constexpr (st == 0) {
            static_for<0, 4>([&](auto KI) { constexpr int ks = KI; fa[ks] = *(const LAS s16x8*)(sl + al + ks * 1024); fw[ks] = wfrag(sl + wl + ks * 2048, sl + wl + ks * 2048 + 1024); });
        }
        static_for<0, 4>([&](auto KI) {
            constexpr int ks = KI;
            SBAR();
            if constexpr (!(st == 21 && ks >= 2)) { if constexpr (st < 22) accF = mfma32(fw[ks], fa[ks], accF); else accS = mfma32(fw[ks], fa[ks], accS); }
            if constexpr (st + 1 < 30) { fa[ks] = *(const LAS s16x8*)(nsl + al + ks * 1024); fw[ks] = wfrag(nsl + wl + ks * 2048, nsl + wl + ks * 2048 + 1024); }
            FS3(2 + ks);
        });
        SBAR();
    });
    raw_barrier();
    const int token = 64 * r64 + 32 * ttl + (lane & 31);
#pragma unroll
    for (int g = 0; g < 4; ++g) {
        int col = 64 * c64 + 32 * ct + 8 * g + 4 * h;
        size_t off = ((size_t)token * 512 + col) * 2;
        uint2 xv = *(const uint2*)(c.x + off);
        float xs[4] = {__uint_as_float(xv.x << 16), __uint_as_float(xv.x & 0xffff0000u), __uint_as_float(xv.y << 16), __uint_as_float(xv.y & 0xffff0000u)};
        float r[4];
#pragma unroll
        for (int i = 0; i < 4; ++i) {
            float fv = accF[4 * g + i], sv = accS[4 * g + i];
            float e = __builtin_amdgcn_exp2f(fv * -1.4426950408889634f);
            float gg = fv * __builtin_amdgcn_rcpf(1.f + e);
            r[i] = xs[i] + sv * gg;
        }
        uint2 ov = {pack_bf16(r[0], r[1]), pack_bf16(r[2], r[3])};
        *(uint2*)(c.out + off) = ov;
    }
}

#define PHASE_MASK 7
template <int PMASK>
__global__ __launch_bounds__(512, 2) void fused_kernel(const char* __restrict__ x, const char* __restrict__ wqkv, const char* __restrict__ wo,
                                                       const char* __restrict__ w1, const char* __restrict__ w3, const char* __restrict__ w2,
                                                       char* out, char* ws) {
    extern __shared__ __attribute__((aligned(16))) char smem[];
    LAS char* sm = (LAS char*)smem;
    Ctx c;
    const int b = blockIdx.x, xcd = b & 7, j = b >> 3;
    c.rb = (xcd >> 2) * 8 + (j & 7);
    const int cg = (xcd & 3) * 4 + (j >> 3);
    c.hp = cg;
    const int np = cg < 11 ? 3 : 2;
    c.pair0 = cg < 11 ? 3 * cg : 33 + 2 * (cg - 11);
    c.w = __builtin_amdgcn_readfirstlane(threadIdx.x >> 6);
    c.rg = c.w & 3;
    c.lane = threadIdx.x & 63;
    c.voff_qkv = (c.lane >> 2) * 3072 + (c.lane & 3) * 16;
    c.voff_ffn = (c.lane >> 2) * 2752 + (c.lane & 3) * 16;
    c.voff_x = (c.lane >> 3) * 1024 + (((c.lane & 7) ^ ((4 * (c.w & 1) + (c.lane >> 4)) & 7)) << 4);
    c.rx = mk_rsrc(x, S * D * 2);
    c.rq = mk_rsrc(wqkv, D * 3 * D * 2);
    c.rf1 = mk_rsrc(w1, D * F * 2); c.rf3 = mk_rsrc(w3, D * F * 2);
    c.rK = mk_rsrc(ws + WS_K, 2u << 20); c.rV = mk_rsrc(ws + WS_V, 2u << 20); c.rA = mk_rsrc(ws + WS_ACT, NTT * NCH * 1024);
    c.rw2 = mk_rsrc(w2, F * D * 2); c.rwo = mk_rsrc(wo, D * D * 2);
    c.sQ = __builtin_amdgcn_make_buffer_rsrc(ws + WS_Q, 0, 2u << 20, 0x00020000);
    c.sK = __builtin_amdgcn_make_buffer_rsrc(ws + WS_K, 0, 2u << 20, 0x00020000);
    c.sV = __builtin_amdgcn_make_buffer_rsrc(ws + WS_V, 0, 2u << 20, 0x00020000);
    c.sA = __builtin_amdgcn_make_buffer_rsrc(ws + WS_ACT, 0, NTT * NCH * 1024, 0x00020000);
    c.x = x; c.out = out; c.ws = ws;
    TSTAMP(0);
    if (c.w == 4) {
        unsigned e = __hip_atomic_load((unsigned*)ws, __ATOMIC_RELAXED, __HIP_MEMORY_SCOPE_AGENT);
        if (c.lane == 0) *(volatile LAS unsigned*)(sm + SCR_TAG) = e + 1u;
    }
    if constexpr (!(PMASK & 1)) { wg_barrier(); c.tag = *(volatile LAS unsigned*)(sm + SCR_TAG); }
    if constexpr (PMASK & 1) {
        if (c.w < 4) { static_for<0, 8>([&](auto I) { if (np == 3) g1_issue_item<0, 3, I>(c, sm); else g1_issue_item<0, 2, I>(c, sm); }); }
        else { static_for<0, 8>([&](auto I) { if (np == 3) g1_issue_item<1, 3, I>(c, sm); else g1_issue_item<1, 2, I>(c, sm); }); }
    }
    if constexpr (PMASK & 1) {
        if (c.w < 4) { if (np == 3) phase_ab<0, 3>(c, sm); else phase_ab<0, 2>(c, sm); }
        else { if (np == 3) phase_ab<1, 3>(c, sm); else phase_ab<1, 2>(c, sm); }
        wait_vm<0>();
        raw_barrier();
        if (threadIdx.x == 0) flag_set(c, WS_HF, c.rb * 16 + c.hp);
    }
    TSTAMP(8);
    if constexpr (PMASK & 2) {
        if (c.w == 0) {
            int r0 = c.rb >= 2 ? c.rb - 2 : 0;
            flags_wait(c, WS_KVF, r0 * 16 + c.hp, 16, c.rb - r0 + 1);
        }
        raw_barrier();
        TSTAMP(9);
        phase_attn(c, sm);
        wait_vm<0>();
        raw_barrier();
        if (threadIdx.x == 0) flag_set(c, WS_OF, c.rb * 16 + c.hp);
    }
    TSTAMP(10);
    if constexpr (PMASK & 4) {
        const int r64 = 4 * xcd + (j >> 3), c64 = j & 7;
        if (c.w == 0) flags_wait(c, WS_HF, (r64 >> 1) * 16, 1, 16);
        raw_barrier();
        TSTAMP(11);
        if (c.w < 4) phase_out_consumer(c, sm, r64, c64); else phase_out_loader(c, sm, r64, c64);
        TSTAMP(12);
        if (b == 0 && c.w == 0) {
            for (int g = 0; g < 4; ++g) flags_wait(c, WS_ARR, g * 64, 1, 64);
            if (c.lane == 0) __hip_atomic_store((unsigned*)ws, c.tag, __ATOMIC_RELAXED, __HIP_MEMORY_SCOPE_AGENT);
        }
    }
}

extern "C" void kernel_launch(void* const* d_in, const int* in_sizes, int n_in, void* d_out, int out_size, void* d_ws, size_t ws_size,
                              hipStream_t stream) {
    const char* x = (const char*)d_in[0];
    const char* wqkv = (const char*)d_in[1];
    const char* wo = (const char*)d_in[2];
    const char* w1 = (const char*)d_in[4];
    const char* w3 = (const char*)d_in[5];
    const char* w2 = (const char*)d_in[6];
    hipFuncSetAttribute((const void*)fused_kernel<7>, hipFuncAttributeMaxDynamicSharedMemorySize, LDS_BYTES);
    fused_kernel<7><<<256, 512, LDS_BYTES, stream>>>(x, wqkv, wo, w1, w3, w2, (char*)d_out, (char*)d_ws);
}
```
